```python
import torch

def _softmax(x, axis=-1):
    return torch.softmax(x, dim=axis)
def _gelu(x):
    return torch.nn.functional.gelu(x, approximate="tanh")
def _silu(x):
    return torch.nn.functional.silu(x)
def _sigmoid(x):
    return torch.sigmoid(x)
def _layernorm(x, axis=-1, eps=1e-5):
    m = x.mean(dim=axis, keepdim=True)
    v = x.var(dim=axis, keepdim=True, unbiased=False)
    return (x - m) / torch.sqrt(v + eps)
def _scatter_add(tgt, idx, src, axis=0):
    return tgt.clone().index_add_(axis, idx, src)
def _segment_sum(src, seg, n):
    out = torch.zeros((n,) + src.shape[1:], dtype=src.dtype)
    return out.index_add_(0, seg, src)
def _pad(x, pw):
    flat = tuple(v for pair in reversed(pw) for v in pair)
    return torch.nn.functional.pad(x, flat)

kernel_name = 'standalone__structured_sparse_n_m__ragged'

def setup_inputs():

    _w = (lambda a: (lambda t: t.to(torch.bfloat16) if t.is_floating_point() else t)(torch.as_tensor(a)))
    B, S, D = 16, 512, 1024
    assert D % 4 == 0, "d_model must be divisible by M=4 for 2:4 sparsity"
    return {
        "x": _w((lambda *s: torch.randn(*s, dtype=torch.bfloat16) * 0.05)(B, S, D)),
        "w": _w((lambda *s: torch.randn(*s, dtype=torch.bfloat16) * 0.05)(D, D)),
        "b": _w((lambda *s: torch.randn(*s, dtype=torch.bfloat16) * 0.05)(D)),
    }

def reference(x, w, b):
    B, S, D = x.shape
    N, M = 2, 4
    G = D // M
    wg = w.reshape(G, M, D)
    mag = torch.abs(wg)
    order = torch.argsort(mag, 1)
    thr = (lambda x, i, axis: torch.take_along_dim(x, i, dim=axis))(mag, order[:, M - N:M - N + 1, :], 1)
    mask = (lambda x, dt: x.to(dt))(mag >= thr, w.dtype)
    wp = (wg * mask).reshape(D, D)
    return torch.einsum("bsd,df->bsf", x, wp) + b
```

```cpp
#include <hip/hip_runtime.h>
#include <hip/hip_bf16.h>
typedef unsigned short u16;
typedef __bf16 bf16x8 __attribute__((ext_vector_type(8)));
typedef float f32x4 __attribute__((ext_vector_type(4)));
typedef unsigned u32x4 __attribute__((ext_vector_type(4)));
typedef unsigned u32x2 __attribute__((ext_vector_type(2)));

constexpr int Mtot = 8192, Ntot = 1024, Ktot = 1024;

__global__ __launch_bounds__(256) void prune_kernel(const u16* __restrict__ w, u16* __restrict__ wt) {
  int t = blockIdx.x * 256 + threadIdx.x;
  int n = t & 1023, k0 = (t >> 10) * 8;
  u16 o[8];
#pragma unroll
  for (int g = 0; g < 2; ++g) {
    unsigned v[4], m[4];
#pragma unroll
    for (int i = 0; i < 4; ++i) { v[i] = w[(size_t)(k0 + g * 4 + i) * Ntot + n]; m[i] = v[i] & 0x7FFF; }
    unsigned hi01 = max(m[0], m[1]), lo01 = min(m[0], m[1]), hi23 = max(m[2], m[3]), lo23 = min(m[2], m[3]);
    unsigned thr = max(min(hi01, hi23), max(lo01, lo23));
#pragma unroll
    for (int i = 0; i < 4; ++i) o[g * 4 + i] = (m[i] >= thr) ? (u16)v[i] : (u16)0;
  }
  u32x4 r; r.x = o[0] | (o[1] << 16); r.y = o[2] | (o[3] << 16); r.z = o[4] | (o[5] << 16); r.w = o[6] | (o[7] << 16);
  *(u32x4*)(wt + (size_t)n * Ktot + k0) = r;
}

constexpr int BM = 128, BK = 64;
__device__ __forceinline__ int swz(int r, int c16) { return r * 128 + ((c16 ^ (r & 7)) << 4); }

__global__ __launch_bounds__(256, 2) void gemm_kernel(const u16* __restrict__ x, const u16* __restrict__ wt, const u16* __restrict__ bias, u16* __restrict__ out) {
  extern __shared__ __attribute__((aligned(16))) char smem[];
  int bid = blockIdx.x;
  int xcd = bid & 7, off = bid >> 3; int wg = xcd * 64 + off;
  int pm = wg >> 3, pn = wg & 7;
  int brow = pm * BM, bcol = pn * BM;
  int tid = threadIdx.x, wid = tid >> 6, lane = tid & 63, wr = wid >> 1, wc = wid & 1, fr = lane & 15, fq = lane >> 4;
  auto stage = [&](int buf, int kt) {
    char* SA = smem + buf * 32768; char* SB = SA + 16384;
#pragma unroll
    for (int i = 0; i < 4; ++i) {
      int b = tid * 16 + i * 4096; int r = b >> 7, c16 = ((b >> 4) & 7) ^ (r & 7);
      __builtin_amdgcn_global_load_lds((const unsigned*)(x + (size_t)(brow + r) * Ktot + kt * BK + c16 * 8), (unsigned*)(SA + b), 16, 0, 2);
      __builtin_amdgcn_global_load_lds((const unsigned*)(wt + (size_t)(bcol + r) * Ktot + kt * BK + c16 * 8), (unsigned*)(SB + b), 16, 0, 0);
    }
  };
  f32x4 acc[4][4] = {};
  stage(0, 0);
  asm volatile("s_waitcnt vmcnt(0)" ::: "memory"); __syncthreads();
  for (int t = 0; t < Ktot / BK; ++t) {
    int cur = t & 1;
    if (t + 1 < Ktot / BK) stage(cur ^ 1, t + 1);
    char* SA = smem + cur * 32768; char* SB = SA + 16384;
#pragma unroll
    for (int kk = 0; kk < 2; ++kk) {
      bf16x8 a[4], b[4];
#pragma unroll
      for (int m = 0; m < 4; ++m) a[m] = *(const bf16x8*)(SA + swz(wr * 64 + m * 16 + fr, kk * 4 + fq));
#pragma unroll
      for (int n = 0; n < 4; ++n) b[n] = *(const bf16x8*)(SB + swz(wc * 64 + n * 16 + fr, kk * 4 + fq));
#pragma unroll
      for (int m = 0; m < 4; ++m)
#pragma unroll
        for (int n = 0; n < 4; ++n)
          acc[m][n] = __builtin_amdgcn_mfma_f32_16x16x32_bf16(b[n], a[m], acc[m][n], 0, 0, 0);
    }
    asm volatile("s_waitcnt vmcnt(0)" ::: "memory"); __syncthreads();
  }
#pragma unroll
  for (int n = 0; n < 4; ++n) {
    int col = bcol + wc * 64 + n * 16 + fq * 4;
    u32x2 bb = *(const u32x2*)(bias + col);
    float b0 = __uint_as_float(bb.x << 16), b1 = __uint_as_float(bb.x & 0xFFFF0000u), b2 = __uint_as_float(bb.y << 16), b3 = __uint_as_float(bb.y & 0xFFFF0000u);
#pragma unroll
    for (int m = 0; m < 4; ++m) {
      int row = brow + wr * 64 + m * 16 + fr;
      float v0 = (float)(__bf16)acc[m][n][0] + b0, v1 = (float)(__bf16)acc[m][n][1] + b1, v2 = (float)(__bf16)acc[m][n][2] + b2, v3 = (float)(__bf16)acc[m][n][3] + b3;
      __bf16 o0 = (__bf16)v0, o1 = (__bf16)v1, o2 = (__bf16)v2, o3 = (__bf16)v3;
      u32x2 o; o.x = __builtin_bit_cast(u16, o0) | ((unsigned)__builtin_bit_cast(u16, o1) << 16); o.y = __builtin_bit_cast(u16, o2) | ((unsigned)__builtin_bit_cast(u16, o3) << 16);
      *(u32x2*)(out + (size_t)row * Ntot + col) = o;
    }
  }
}

extern "C" void kernel_launch(void* const* d_in, const int* in_sizes, int n_in, void* d_out, int out_size, void* d_ws, size_t ws_size, hipStream_t stream) {
  const u16* x = (const u16*)d_in[0]; const u16* w = (const u16*)d_in[1]; const u16* b = (const u16*)d_in[2];
  u16* wt = (u16*)d_ws;
  prune_kernel<<<512, 256, 0, stream>>>(w, wt);
  hipFuncSetAttribute((const void*)gemm_kernel, hipFuncAttributeMaxDynamicSharedMemorySize, 65536);
  gemm_kernel<<<512, 256, 65536, stream>>>(x, wt, b, (u16*)d_out);
}
```

```cpp
#include <hip/hip_runtime.h>
typedef unsigned short u16;
typedef unsigned long long u64;
typedef __bf16 bf16x8 __attribute__((ext_vector_type(8)));
typedef __bf16 bf16x16 __attribute__((ext_vector_type(16)));
typedef __bf16 bf16x2 __attribute__((ext_vector_type(2)));
typedef float f32x2 __attribute__((ext_vector_type(2)));
typedef float f32x16 __attribute__((ext_vector_type(16)));
typedef unsigned u32x2 __attribute__((ext_vector_type(2)));
typedef unsigned u32x4 __attribute__((ext_vector_type(4)));
typedef unsigned u32x8 __attribute__((ext_vector_type(8)));
typedef unsigned short u16x2 __attribute__((ext_vector_type(2)));
typedef short s16x2 __attribute__((ext_vector_type(2)));
typedef __attribute__((address_space(1))) unsigned gu32;
typedef __attribute__((address_space(1))) u64 gu64;

constexpr int Ntot = 1024, Ktot = 1024;
constexpr int PC_VAL = 0  , PC_IDX = 8192  , PC_TR = 9216  , PC_SM = 9280  ;
constexpr int PC_S2V = 10240, PC_SIZE = 18432;
constexpr int PL_WT = 16384, PL_CT = 16400, PL_DN = 16404;
constexpr int WS_COPY = 128 * PC_SIZE;
constexpr int WS_FLAG = 2 * WS_COPY, WS_EPOCH = WS_FLAG + 4096;
constexpr int SC1 = 16;
#define DEPTH_ 7
constexpr int DEPTH = DEPTH_;

#define STAMP(i)
#define STAMP_FLUSH(n)

__device__ __forceinline__ unsigned pk_max(unsigned a, unsigned b) { return __builtin_bit_cast(unsigned, __builtin_elementwise_max(__builtin_bit_cast(u16x2, a), __builtin_bit_cast(u16x2, b))); }
__device__ __forceinline__ unsigned pk_min(unsigned a, unsigned b) { return __builtin_bit_cast(unsigned, __builtin_elementwise_min(__builtin_bit_cast(u16x2, a), __builtin_bit_cast(u16x2, b))); }
__device__ __forceinline__ unsigned pk_ltmask(unsigned a, unsigned b) {
  s16x2 d = __builtin_bit_cast(s16x2, a) - __builtin_bit_cast(s16x2, b); d = d >> (s16x2){15, 15}; return __builtin_bit_cast(unsigned, d); }
__device__ __forceinline__ unsigned bfi(unsigned m, unsigned x, unsigned y) { return (m & x) | (~m & y); }
__device__ __forceinline__ void wg_barrier() {
  asm volatile("s_waitcnt lgkmcnt(0)" ::: "memory"); __builtin_amdgcn_s_barrier(); asm volatile("" ::: "memory"); }

#define SB __builtin_amdgcn_sched_barrier(0)
#define SMF(A, B, C, I, AB) __builtin_amdgcn_smfmac_f32_32x32x32_bf16(__builtin_bit_cast(bf16x8, A), __builtin_bit_cast(bf16x16, B), C, (int)(I), 0, AB)
#define LDW(off, soff) __builtin_amdgcn_raw_buffer_load_b128(rs, off, soff, SC1)
#define LDI(off, soff) __builtin_amdgcn_raw_buffer_load_b32(rs, off, soff, SC1)

__device__ __forceinline__ u32x8 ldB(const char* smem, unsigned a0, unsigned a1, int off) {
  const u32x4 lo = *(const u32x4*)(smem + a0 + off), hi = *(const u32x4*)(smem + a1 + off);
  return (u32x8){lo.x, lo.y, lo.z, lo.w, hi.x, hi.y, hi.z, hi.w};
}

__global__ __launch_bounds__(512, 2) void sp24_gemm(const u16* __restrict__ x, const u16* __restrict__ w, const u16* __restrict__ bias,
                                                    u16* __restrict__ out, char* ws) {
  extern __shared__ __attribute__((aligned(16))) char smem[];
  const int bid = blockIdx.x, xcd = bid & 7, jj = bid >> 3;
  const int mt = xcd * 16 + (jj >> 1), nh = jj & 1, cp = xcd >> 2;
  const int grp = cp * 2 + nh, rank = (xcd & 3) * 16 + (jj >> 1);
  const int tid = threadIdx.x, lane = tid & 63;
  const int wv = __builtin_amdgcn_readfirstlane(tid >> 6);
  STAMP(0);
  const int gp = (tid >> 3) & 31, o = tid & 7;
  const u16* wsrc = w + (size_t)(gp * 8) * Ntot + nh * 512 + o * 4;
  u32x2 r[8];
  if (wv < 4) {
    const u16* src = wsrc + (size_t)((rank & 3) * 256) * Ntot + (rank >> 2) * 32;
#pragma unroll
    for (int i = 0; i < 8; ++i) r[i] = __builtin_nontemporal_load((const u32x2*)(src + (size_t)i * Ntot));
  }
  SB;
  char* img = smem + 131072;
  if (tid == 0) *(u32x2*)(img + PL_CT) = (u32x2){0u, 0u};
  const int ml = lane & 31, hb = lane >> 5;
  gu64* flags = (gu64*)(ws + WS_FLAG + grp * 512);
  gu32* epoch = (gu32*)(ws + WS_EPOCH + grp * 256);
  unsigned ep = __hip_atomic_load(epoch, __ATOMIC_RELAXED, __HIP_MEMORY_SCOPE_AGENT);
  const int NB0 = nh * 16 + wv * 2;
  u32x4 bq[2][2];
#pragma unroll
  for (int nb = 0; nb < 2; ++nb) { const u32x4* bp = (const u32x4*)(bias + (NB0 + nb) * 32 + hb * 16); bq[nb][0] = bp[0]; bq[nb][1] = bp[1]; }
  wg_barrier();
  const __amdgpu_buffer_rsrc_t rs = __builtin_amdgcn_make_buffer_rsrc((void*)(ws + (size_t)cp * WS_COPY + (size_t)nh * 64 * PC_SIZE), 0, 64 * PC_SIZE, 0x00020000);
  const unsigned l16 = lane * 16;
  char* stg = img + wv * 4096;
  if (wv >= 4) {
    const u16* xb = x + (size_t)(mt * 64 + (wv - 4) * 16) * Ktot;
#pragma unroll 4
    for (int i = 0; i < 16; ++i) {
      const u16* src = xb + (size_t)i * Ktot + ((lane ^ i) << 3);
      char* dst = smem + ((wv - 4) * 16 + i) * 1024;
      __builtin_amdgcn_global_load_lds((const unsigned*)src, (unsigned*)dst, 16, 0, 2);
      __builtin_amdgcn_global_load_lds((const unsigned*)(src + 512), (unsigned*)(dst + 65536), 16, 0, 2);
    }
  }
  u64 ready = 0;
  int todo = rank;
  unsigned round = 0;
  for (;;) {
    round += 4;
    if (wv < 4) {
      const int kq = todo & 3;
      const int pdst = todo * PC_SIZE;
      const int sw = (gp >> 2) & 1, h = (gp >> 1) & 1, jp = gp & 1;
      const int rho0 = 8 * (o & 3) + 4 * (o >> 2);
      const int fl0 = rho0 + 32 * h;
      unsigned emask = 0;
      unsigned V0[2][2], V1[2][2], NI[2][2];
#pragma unroll
      for (int g = 0; g < 2; ++g)
#pragma unroll
        for (int pp = 0; pp < 2; ++pp) {
          const unsigned a = r[g * 4 + 0][pp], b = r[g * 4 + 1][pp], c = r[g * 4 + 2][pp], d = r[g * 4 + 3][pp];
          const unsigned ma = a & 0x7FFF7FFFu, mb = b & 0x7FFF7FFFu, mc = c & 0x7FFF7FFFu, md = d & 0x7FFF7FFFu;
          const unsigned h01 = pk_max(ma, mb), l01 = pk_min(ma, mb), h23 = pk_max(mc, md), l23 = pk_min(mc, md);
          unsigned thr = pk_max(pk_min(h01, h23), pk_max(l01, l23));
          thr = pk_max(thr, 0x00010001u);
          const unsigned na = pk_ltmask(ma, thr), nb = pk_ltmask(mb, thr), nc = pk_ltmask(mc, thr), nd = pk_ltmask(md, thr);
          const unsigned v0 = bfi(na, bfi(nb, c, b), a);
          unsigned v1 = bfi(nd, bfi(nc, b, c), d);
          v1 &= ~(na & nd & (nb ^ nc));
          const unsigned p0 = na & bfi(nb, 0x00020002u, 0x00010001u);
          const unsigned p1 = bfi(nd, bfi(nc, 0x00040004u, 0x00080008u), 0x000C000Cu);
          V0[g][pp] = v0; V1[g][pp] = v1; NI[g][pp] = p0 | p1;
          const unsigned eb = ~nb & ~na & ~(nc & nd), ec = ~nc & ~nd & ~(na & nb);
          const unsigned e2 = (eb & 0x00020001u) | (ec & 0x00080004u);
          emask |= ((e2 | (e2 >> 16)) & 15u) << (g * 8 + pp * 4);
        }
      char* fp = stg + sw * 1024 + fl0 * 16 + jp * 8;
#pragma unroll
      for (int pp = 0; pp < 2; ++pp) {
        u32x2 lo, hi;
        lo.x = __builtin_amdgcn_perm(V1[0][pp], V0[0][pp], 0x05040100u); lo.y = __builtin_amdgcn_perm(V1[1][pp], V0[1][pp], 0x05040100u);
        hi.x = __builtin_amdgcn_perm(V1[0][pp], V0[0][pp], 0x07060302u); hi.y = __builtin_amdgcn_perm(V1[1][pp], V0[1][pp], 0x07060302u);
        *(u32x2*)(fp + pp * 32) = lo; *(u32x2*)(fp + pp * 32 + 16) = hi;
        const unsigned by = NI[0][pp] | (NI[1][pp] << 4);
        char* q = stg + 2048 + (fl0 + 2 * pp) * 4 + sw * 2 + jp;
        *(unsigned char*)q = (unsigned char)by; *(unsigned char*)(q + 4) = (unsigned char)(by >> 16);
      }
      int tot = 0;
      for (unsigned rem = emask;;) { const u64 bl = __ballot(rem != 0); if (!bl) break; tot += __popcll(bl); rem &= rem - 1; }
      if (lane == 0) { *(int*)(img + PL_WT + wv * 4) = tot;
        asm volatile("s_waitcnt lgkmcnt(0)" ::: "memory");
        __hip_atomic_fetch_add((unsigned*)(img + PL_CT), 1u, __ATOMIC_RELAXED, __HIP_MEMORY_SCOPE_WORKGROUP); }
      __builtin_amdgcn_raw_buffer_store_b128(*(const u32x4*)(stg + l16), rs, PC_VAL + wv * 2048 + l16, pdst, SC1);
      __builtin_amdgcn_raw_buffer_store_b128(*(const u32x4*)(stg + 1024 + l16), rs, PC_VAL + wv * 2048 + 1024 + l16, pdst, SC1);
      __builtin_amdgcn_raw_buffer_store_b32(*(const unsigned*)(stg + 2048 + lane * 4), rs, PC_IDX + wv * 256 + lane * 4, pdst, SC1);
      STAMP(1);
      while (__hip_atomic_load((unsigned*)(img + PL_CT), __ATOMIC_RELAXED, __HIP_MEMORY_SCOPE_WORKGROUP) < round) {}
      asm volatile("" ::: "memory");
      const u32x4 wt = *(const u32x4*)(img + PL_WT);
      const int total = (int)(wt.x + wt.y + wt.z + wt.w);
      if (wv == 0 && lane >= total && lane < 16) __builtin_amdgcn_raw_buffer_store_b32(0u, rs, PC_TR + lane * 4, pdst, SC1);
      unsigned s2mask = 0;
      int base = (wv > 0 ? (int)wt.x : 0) + (wv > 1 ? (int)wt.y : 0) + (wv > 2 ? (int)wt.z : 0);
      for (unsigned rem = emask;;) {
        const u64 bl = __ballot(rem != 0); if (!bl) break;
        if (rem != 0) {
          const int q = base + __builtin_amdgcn_mbcnt_hi((unsigned)(bl >> 32), __builtin_amdgcn_mbcnt_lo((unsigned)bl, 0));
          const int bit = __builtin_ctz(rem);
          if (q < 16) {
            const int g = bit >> 3, pp = (bit >> 2) & 1, e = (bit >> 1) & 1, hf = bit & 1;
            unsigned dv = 0;
#pragma unroll
            for (int gg = 0; gg < 2; ++gg)
#pragma unroll
              for (int ee = 0; ee < 2; ++ee)
#pragma unroll
                for (int p2 = 0; p2 < 2; ++p2) dv = (g == gg && e == ee && pp == p2) ? r[gg * 4 + 1 + ee][p2] : dv;
            const unsigned val = hf ? (dv >> 16) : (dv & 0xFFFFu);
            const unsigned gi = (unsigned)((kq & 1) * 64 + gp * 2 + g);
            const unsigned rho = (unsigned)(rho0 + 2 * pp + hf);
            __builtin_amdgcn_raw_buffer_store_b32((val << 16) | ((unsigned)e << 15) | (rho << 10) | (gi << 3), rs, PC_TR + q * 4, pdst, SC1);
          } else s2mask |= 1u << bit;
        }
        base += __popcll(bl); rem &= rem - 1;
      }
      unsigned stepm = 0;
      if (total > 16) {
        const u64 bl = __ballot(s2mask != 0);
        stepm = ((unsigned)bl ? 1u : 0u) | ((unsigned)(bl >> 32) ? 2u : 0u);
#pragma unroll
        for (int pp = 0; pp < 2; ++pp) {
          unsigned wv2[2][2];
#pragma unroll
          for (int g = 0; g < 2; ++g) {
            const unsigned bb = r[g * 4 + 1][pp], cc = r[g * 4 + 2][pp];
            const unsigned bits = (s2mask >> (g * 8 + pp * 4)) & 15u;
            wv2[0][g] = ((bits & 1u) ? (bb & 0xFFFFu) : 0u) | ((bits & 4u) ? (cc << 16) : 0u);
            wv2[1][g] = ((bits & 2u) ? (bb >> 16) : 0u) | ((bits & 8u) ? (cc & 0xFFFF0000u) : 0u);
          }
          *(u32x2*)(fp + pp * 32) = (u32x2){wv2[0][0], wv2[0][1]}; *(u32x2*)(fp + pp * 32 + 16) = (u32x2){wv2[1][0], wv2[1][1]};
        }
        __builtin_amdgcn_raw_buffer_store_b128(*(const u32x4*)(stg + l16), rs, PC_S2V + wv * 2048 + l16, pdst, SC1);
        __builtin_amdgcn_raw_buffer_store_b128(*(const u32x4*)(stg + 1024 + l16), rs, PC_S2V + wv * 2048 + 1024 + l16, pdst, SC1);
      }
      if (lane == 0) __builtin_amdgcn_raw_buffer_store_b8((unsigned char)stepm, rs, PC_SM + wv, pdst, SC1);
      asm volatile("s_waitcnt vmcnt(0)" : "+v"(ep) :: "memory");
      STAMP(2);
      const unsigned t0 = ep + 1u;
      const u64 tag = ((u64)(t0 * 0x9E3779B1u ^ 0x7F4A7C15u) << 32) | t0;
      if (lane == 0) {
        const unsigned dn = __hip_atomic_fetch_add((unsigned*)(img + PL_DN), 1u, __ATOMIC_RELAXED, __HIP_MEMORY_SCOPE_WORKGROUP);
        if (dn + 1u == round) __hip_atomic_store(flags + todo, tag, __ATOMIC_RELAXED, __HIP_MEMORY_SCOPE_AGENT);
      }
      if (wv == 0) {
        if (round > 4) ready |= 1ull << todo;
#pragma unroll 1
        for (int spin = 0; spin < 4096; ++spin) {
          const u64 f = __hip_atomic_load(flags + lane, __ATOMIC_RELAXED, __HIP_MEMORY_SCOPE_AGENT);
          ready |= __ballot(f == tag);
          if (ready == ~0ull) break;
          __builtin_amdgcn_s_sleep(1);
        }
        ready |= 1ull << todo;
        const int nx = ready == ~0ull ? -1 : __builtin_ctzll(~ready);
        if (lane < 8) *(int*)(img + lane * 4096 + 4092) = nx;
        if (nx < 0 && lane == 0) __hip_atomic_fetch_add(epoch, 1u, __ATOMIC_RELAXED, __HIP_MEMORY_SCOPE_AGENT);
      }
    } else {
      STAMP(1);
      asm volatile("s_waitcnt vmcnt(0)" ::: "memory");
      STAMP(2);
    }
    wg_barrier();
    todo = __builtin_amdgcn_readfirstlane(*(const int*)(stg + 4092));
    if (todo < 0) break;
    if (wv < 4) {
      const u16* src = wsrc + (size_t)((todo & 3) * 256) * Ntot + (todo >> 2) * 32;
#pragma unroll
      for (int i = 0; i < 8; ++i) r[i] = __builtin_nontemporal_load((const u32x2*)(src + (size_t)i * Ntot));
    }
    wg_barrier();
  }
  __builtin_amdgcn_fence(__ATOMIC_ACQUIRE, "wavefront");
  STAMP(3);

  const int pb0 = wv * 8 * PC_SIZE;
  const bool late = wv >= 4;
  f32x16 acc[2][2];
#pragma unroll
  for (int nb = 0; nb < 2; ++nb) {
    const u32x4 b0 = bq[nb][0], b1 = bq[nb][1];
    const unsigned bw[8] = {b0.x, b0.y, b0.z, b0.w, b1.x, b1.y, b1.z, b1.w};
#pragma unroll
    for (int i = 0; i < 8; ++i) { acc[nb][0][2 * i] = __uint_as_float(bw[i] << 16); acc[nb][0][2 * i + 1] = __uint_as_float(bw[i] & 0xFFFF0000u); }
    acc[nb][1] = acc[nb][0];
  }
  const unsigned rowb = ml * 1024;
  const unsigned u = (unsigned)(hb ^ (ml & 15));

  auto tie_phase = [&]() __attribute__((always_inline)) {
      unsigned rec[2];
      rec[0] = LDI((lane >> 4) * PC_SIZE + PC_TR + (lane & 15) * 4, pb0);
      rec[1] = LDI((lane >> 4) * PC_SIZE + PC_TR + (lane & 15) * 4, pb0 + 4 * PC_SIZE);
      const unsigned smk = LDI((lane & 7) * PC_SIZE + PC_SM, pb0);
      *(u32x4*)(stg + l16) = (u32x4){0, 0, 0, 0};
      *(u32x4*)(stg + 1024 + l16) = (u32x4){0, 0, 0, 0};
      {
        const unsigned tpos = 2048 + ((lane >> 4) * 16 + ((lane >> 2) & 1) * 8 + ((lane >> 3) & 1) * 4 + (lane & 3)) * 4;
        *(unsigned*)(stg + tpos) = rec[0] & 0x3F8u;
        *(unsigned*)(stg + tpos + 256) = rec[1] & 0x3F8u;
      }
      const unsigned apos = (unsigned)(((lane >> 4) & 1) * 1024 + ((lane >> 3) & 1) * 512 + (lane & 7) * 2);
      const unsigned tb = rowb + (unsigned)((ml & 15) << 4);
      u64 cls[2]; cls[0] = __ballot((rec[0] >> 15) & 1u); cls[1] = __ballot((rec[1] >> 15) & 1u);
      unsigned tbm[2][2];
      tbm[0][0] = tb; tbm[0][1] = tb + 32768u; tbm[1][0] = tb + 65536u; tbm[1][1] = tb + 98304u;
      asm("" : "+v"(tbm[0][1])); asm("" : "+v"(tbm[1][0])); asm("" : "+v"(tbm[1][1]));
      u32x2 g[2][8]; u32x4 at[2]; u32x8 ko;
      { const u32x4 k0 = *(const u32x4*)(stg + 2048 + hb * 32), k1 = *(const u32x4*)(stg + 2048 + hb * 32 + 16);
        ko = (u32x8){k0.x, k0.y, k0.z, k0.w, k1.x, k1.y, k1.z, k1.w}; }
#pragma unroll
      for (int un = 0; un <= 16; ++un) {
        SB;
        if (un < 16) {
          const int pc = un >> 1, mb = un & 1, nb = pc >> 2, bch = (pc >> 1) & 1;
          u16* ap = (u16*)(stg + apos + ((rec[nb] >> 10) & 31u) * 16);
          if ((un & 3) == 0 && hb == bch) *ap = (u16)(rec[nb] >> 16);
#pragma unroll
          for (int j = 0; j < 8; ++j) g[un & 1][j] = *(const u32x2*)(smem + (tbm[bch][mb] ^ ko[j]));
          if (mb == 0) at[pc & 1] = *(const u32x4*)(stg + (pc & 1) * 1024 + l16);
          if ((un & 3) == 2 && hb == bch) *ap = (u16)0;
          if (mb == 1 && pc < 7) {
            const u32x4 k0 = *(const u32x4*)(stg + 2048 + (pc + 1) * 64 + hb * 32), k1 = *(const u32x4*)(stg + 2048 + (pc + 1) * 64 + hb * 32 + 16);
            ko = (u32x8){k0.x, k0.y, k0.z, k0.w, k1.x, k1.y, k1.z, k1.w};
          }
        }
        SB;
        if (un > 0) {
          const int up = un - 1, pp = up >> 1, mb = up & 1, nb = pp >> 2;
          const unsigned c16 = (unsigned)(cls[nb] >> (16 * (pp & 3))) & 0xFFFFu;
          u64 rep; asm("s_bitreplicate_b64_b32 %0, %1" : "=s"(rep) : "s"(c16));
          const unsigned iw = ((unsigned)rep & 0x55555555u) | 0x88888888u;
          const unsigned ti = iw >> (16 * hb);
          u32x8 bt;
#pragma unroll
          for (int j = 0; j < 8; ++j) bt[j] = __builtin_amdgcn_alignbit(g[up & 1][j].y, g[up & 1][j].x, 16);
          acc[nb][mb] = SMF(at[pp & 1], bt, acc[nb][mb], ti, 0);
        }
      }
      SB;
      if (__ballot(smk != 0u)) {
#pragma unroll
        for (int nb = 0; nb < 2; ++nb) {
#pragma unroll 1
          for (int kq = 0; kq < 4; ++kq) {
            unsigned sm = (unsigned)__builtin_amdgcn_readlane((int)smk, nb * 4 + kq);
            while (sm) {
              const int bit = __builtin_ctz(sm); sm &= sm - 1;
              const int st = (bit >> 3) * 2 + (bit & 1);
              const u32x4 a2 = LDW(l16 + PC_S2V, pb0 + (nb * 4 + kq) * PC_SIZE + st * 1024);
              const unsigned c0 = (unsigned)(4 * (st & 3));
              const unsigned xb = rowb + (unsigned)((kq >> 1) * 65536 + (kq & 1) * 512 + (st >> 2) * 256);
              const unsigned a0 = xb + ((c0 ^ u) << 4), a1 = xb + (((c0 + 2) ^ u) << 4);
#pragma unroll
              for (int mb = 0; mb < 2; ++mb) {
                const u32x8 b = ldB(smem, a0, a1, mb * 32768);
                acc[nb][mb] = SMF(a2, b, acc[nb][mb], 0x99999999u, 0);
              }
            }
          }
        }
      }
  };
  if (late) tie_phase();
  STAMP(4);
  {
      __builtin_amdgcn_s_setprio(1);
      const int kq0 = (mt >> 1) & 3;
      const unsigned l4 = lane * 4 + PC_IDX;
      u32x4 a[8][2]; unsigned ix[4][2];
      {
        const int pb = pb0 + kq0 * PC_SIZE;
#pragma unroll
        for (int s = 0; s < DEPTH; ++s) {
          if ((s & 1) == 0) { ix[s >> 1][0] = LDI(l4 + (s >> 1) * 256, pb); ix[s >> 1][1] = LDI(l4 + (s >> 1) * 256, pb + 4 * PC_SIZE); }
#pragma unroll
          for (int nb = 0; nb < 2; ++nb) a[s][nb] = LDW(l16 + s * 1024, pb + nb * 4 * PC_SIZE);
        }
      }
      u32x8 bc[2];
      {
        const unsigned xb = rowb + (unsigned)((kq0 >> 1) * 65536 + (kq0 & 1) * 512);
        const unsigned a0 = xb + (u << 4), a1 = xb + ((2u ^ u) << 4);
        bc[0] = ldB(smem, a0, a1, 0); bc[1] = ldB(smem, a0, a1, 32768);
      }
#define MM(mb, nb) do { if (s & 1) acc[nb][mb] = SMF(a[s][nb], bc[mb], acc[nb][mb], ix[s >> 1][nb], 1); else acc[nb][mb] = SMF(a[s][nb], bc[mb], acc[nb][mb], ix[s >> 1][nb], 0); } while (0)
#define STEP(LAST) \
      _Pragma("unroll") for (int s = 0; s < 8; ++s) { \
        const int sp = s + DEPTH, sn = (s + 1) & 7, pq = sp < 8 ? pb : pbn; \
        const bool pf = sp < 8 || !(LAST); \
        const unsigned a0 = s < 7 ? xa[2 * (sn & 3)] : xn0, a1 = s < 7 ? xa[2 * (sn & 3) + 1] : xn1; \
        u32x8 bn[2]; u32x4 an[2]; unsigned in[2]; \
        SB; \
        MM(0, 0); \
        if (s < 7 || !(LAST)) bn[0] = ldB(smem, a0, a1, (sn >> 2) * 256); \
        SB; \
        MM(0, 1); \
        if (s < 7 || !(LAST)) bn[1] = ldB(smem, a0, a1, (sn >> 2) * 256 + 32768); \
        SB; \
        MM(1, 0); \
        if (pf && (sp & 1) == 0) { in[0] = LDI(l4 + ((sp & 7) >> 1) * 256, pq); in[1] = LDI(l4 + ((sp & 7) >> 1) * 256, pq + 4 * PC_SIZE); } \
        if (pf) an[0] = LDW(l16 + (sp & 7) * 1024, pq); \
        SB; \
        MM(1, 1); \
        if (pf) an[1] = LDW(l16 + (sp & 7) * 1024, pq + 4 * PC_SIZE); \
        if (pf) { a[sp & 7][0] = an[0]; a[sp & 7][1] = an[1]; } \
        if (pf && (sp & 1) == 0) { ix[(sp & 7) >> 1][0] = in[0]; ix[(sp & 7) >> 1][1] = in[1]; } \
        if (s < 7 || !(LAST)) { bc[0] = bn[0]; bc[1] = bn[1]; } \
      }
#define ITER_SETUP \
        const unsigned xb = rowb + (unsigned)((kq >> 1) * 65536 + (kq & 1) * 512); \
        const unsigned xbn = rowb + (unsigned)((kqn >> 1) * 65536 + (kqn & 1) * 512); \
        unsigned xa[8]; _Pragma("unroll") for (int c = 0; c < 8; ++c) xa[c] = xb + (((unsigned)(2 * c) ^ u) << 4); \
        const unsigned xn0 = xbn + (u << 4), xn1 = xbn + ((2u ^ u) << 4);
#pragma unroll 1
      for (int it = 0; it < 3; ++it) {
        const int kq = (kq0 + it) & 3, kqn = (kq + 1) & 3;
        const int pb = pb0 + kq * PC_SIZE, pbn = pb0 + kqn * PC_SIZE;
        ITER_SETUP
        STEP(false)
      }
      {
        const int kq = (kq0 + 3) & 3, kqn = kq;
        const int pb = pb0 + kq * PC_SIZE, pbn = pb;
        ITER_SETUP
        STEP(true)
      }
      SB;
      __builtin_amdgcn_s_setprio(0);
  }
  STAMP(5);
  if (!late) tie_phase();
  {
    u16* ob = out + (size_t)(mt * 64) * Ntot + NB0 * 32;
#pragma unroll
    for (int mb = 0; mb < 2; ++mb) {
#pragma unroll
      for (int nb = 0; nb < 2; ++nb)
#pragma unroll
        for (int hf = 0; hf < 2; ++hf) {
          u32x4 v;
#pragma unroll
          for (int i = 0; i < 4; ++i) {
            const f32x2 f = {acc[nb][mb][hf * 8 + 2 * i], acc[nb][mb][hf * 8 + 2 * i + 1]};
            v[i] = __builtin_bit_cast(unsigned, __builtin_convertvector(f, bf16x2));
          }
          const int ch = nb * 4 + hb * 2 + hf;
          *(u32x4*)(stg + ml * 128 + ((ch ^ (ml & 7)) << 4)) = v;
        }
#pragma unroll
      for (int i = 0; i < 4; ++i) {
        const int row = (lane >> 3) + 8 * i;
        const u32x4 v = *(const u32x4*)(stg + row * 128 + (((lane & 7) ^ (row & 7)) << 4));
        __builtin_nontemporal_store(v, (u32x4*)(ob + (size_t)(mb * 32 + row) * Ntot + (lane & 7) * 8));
      }
    }
  }
  STAMP(6);
  STAMP(7);
  STAMP_FLUSH(8);
}

extern "C" void kernel_launch(void* const* d_in, const int* in_sizes, int n_in, void* d_out, int out_size, void* d_ws, size_t ws_size, hipStream_t stream) {
  (void)hipFuncSetAttribute((const void*)sp24_gemm, hipFuncAttributeMaxDynamicSharedMemorySize, 163840);
  sp24_gemm<<<256, 512, 163840, stream>>>((const u16*)d_in[0], (const u16*)d_in[1], (const u16*)d_in[2], (u16*)d_out, (char*)d_ws);
}
```

```cpp
#define PROBE -1
#include <hip/hip_runtime.h>
typedef unsigned short u16;
typedef unsigned long long u64;
typedef __bf16 bf16x8 __attribute__((ext_vector_type(8)));
typedef __bf16 bf16x16 __attribute__((ext_vector_type(16)));
typedef __bf16 bf16x2 __attribute__((ext_vector_type(2)));
typedef float f32x2 __attribute__((ext_vector_type(2)));
typedef float f32x16 __attribute__((ext_vector_type(16)));
typedef unsigned u32x2 __attribute__((ext_vector_type(2)));
typedef unsigned u32x4 __attribute__((ext_vector_type(4)));
typedef unsigned u32x8 __attribute__((ext_vector_type(8)));
typedef unsigned short u16x2 __attribute__((ext_vector_type(2)));
typedef short s16x2 __attribute__((ext_vector_type(2)));
typedef __attribute__((address_space(1))) unsigned gu32;
typedef __attribute__((address_space(1))) u64 gu64;

constexpr int Ntot = 1024, Ktot = 1024;
constexpr int PC_VAL = 0  , PC_IDX = 8192  , PC_TR = 9216  , PC_SM = 9280  ;
constexpr int PC_S2V = 10240, PC_SIZE = 18432;
constexpr int PL_WT = 16384, PL_CT = 16400, PL_DN = 16404;
constexpr int WS_COPY = 128 * PC_SIZE;
constexpr int WS_FLAG = 2 * WS_COPY, WS_EPOCH = WS_FLAG + 4096;
constexpr int SC1 = 16;
#define DEPTH_ 7
constexpr int DEPTH = DEPTH_;

#define STAMPS 1
#define STAMP(i) do { __builtin_amdgcn_sched_barrier(0); asm volatile("s_memrealtime %0\n\ts_memtime %1\n\ts_waitcnt lgkmcnt(0)" : "=s"(st_t[i]), "=s"(st_c[i]) :: "memory"); __builtin_amdgcn_sched_barrier(0); } while (0)
#define STAMP_FLUSH(n) do { if (PROBE >= 0 && wv < 4) { const u64 d_ = (st_t[PROBE < 0 ? 0 : PROBE] - st_t[0]) * 2; const u64 t0_ = __builtin_amdgcn_s_memrealtime(); while (__builtin_amdgcn_s_memrealtime() - t0_ < d_) __builtin_amdgcn_s_sleep(1); } } while (0)

__device__ __forceinline__ unsigned pk_max(unsigned a, unsigned b) { return __builtin_bit_cast(unsigned, __builtin_elementwise_max(__builtin_bit_cast(u16x2, a), __builtin_bit_cast(u16x2, b))); }
__device__ __forceinline__ unsigned pk_min(unsigned a, unsigned b) { return __builtin_bit_cast(unsigned, __builtin_elementwise_min(__builtin_bit_cast(u16x2, a), __builtin_bit_cast(u16x2, b))); }
__device__ __forceinline__ unsigned pk_ltmask(unsigned a, unsigned b) {
  s16x2 d = __builtin_bit_cast(s16x2, a) - __builtin_bit_cast(s16x2, b); d = d >> (s16x2){15, 15}; return __builtin_bit_cast(unsigned, d); }
__device__ __forceinline__ unsigned bfi(unsigned m, unsigned x, unsigned y) { return (m & x) | (~m & y); }
__device__ __forceinline__ void wg_barrier() {
  asm volatile("s_waitcnt lgkmcnt(0)" ::: "memory"); __builtin_amdgcn_s_barrier(); asm volatile("" ::: "memory"); }

#define SB __builtin_amdgcn_sched_barrier(0)
#define SMF(A, B, C, I, AB) __builtin_amdgcn_smfmac_f32_32x32x32_bf16(__builtin_bit_cast(bf16x8, A), __builtin_bit_cast(bf16x16, B), C, (int)(I), 0, AB)
#define LDW(off, soff) __builtin_amdgcn_raw_buffer_load_b128(rs, off, soff, SC1)
#define LDI(off, soff) __builtin_amdgcn_raw_buffer_load_b32(rs, off, soff, SC1)

__device__ __forceinline__ u32x8 ldB(const char* smem, unsigned a0, unsigned a1, int off) {
  const u32x4 lo = *(const u32x4*)(smem + a0 + off), hi = *(const u32x4*)(smem + a1 + off);
  return (u32x8){lo.x, lo.y, lo.z, lo.w, hi.x, hi.y, hi.z, hi.w};
}

__global__ __launch_bounds__(512, 2) void sp24_gemm(const u16* __restrict__ x, const u16* __restrict__ w, const u16* __restrict__ bias,
                                                    u16* __restrict__ out, char* ws) {
  extern __shared__ __attribute__((aligned(16))) char smem[];
  const int bid = blockIdx.x, xcd = bid & 7, jj = bid >> 3;
  const int mt = xcd * 16 + (jj >> 1), nh = jj & 1, cp = xcd >> 2;
  const int grp = cp * 2 + nh, rank = (xcd & 3) * 16 + (jj >> 1);
  const int tid = threadIdx.x, lane = tid & 63;
  const int wv = __builtin_amdgcn_readfirstlane(tid >> 6);
  u64 st_t[8], st_c[8];
  for (int i_ = 0; i_ < 8; ++i_) { st_t[i_] = 0; st_c[i_] = 0; }
  STAMP(0);
  const int gp = (tid >> 3) & 31, o = tid & 7;
  const u16* wsrc = w + (size_t)(gp * 8) * Ntot + nh * 512 + o * 4;
  u32x2 r[8];
  if (wv < 4) {
    const u16* src = wsrc + (size_t)((rank & 3) * 256) * Ntot + (rank >> 2) * 32;
#pragma unroll
    for (int i = 0; i < 8; ++i) r[i] = __builtin_nontemporal_load((const u32x2*)(src + (size_t)i * Ntot));
  }
  SB;
  char* img = smem + 131072;
  if (tid == 0) *(u32x2*)(img + PL_CT) = (u32x2){0u, 0u};
  const int ml = lane & 31, hb = lane >> 5;
  gu64* flags = (gu64*)(ws + WS_FLAG + grp * 512);
  gu32* epoch = (gu32*)(ws + WS_EPOCH + grp * 256);
  unsigned ep = __hip_atomic_load(epoch, __ATOMIC_RELAXED, __HIP_MEMORY_SCOPE_AGENT);
  const int NB0 = nh * 16 + wv * 2;
  u32x4 bq[2][2];
#pragma unroll
  for (int nb = 0; nb < 2; ++nb) { const u32x4* bp = (const u32x4*)(bias + (NB0 + nb) * 32 + hb * 16); bq[nb][0] = bp[0]; bq[nb][1] = bp[1]; }
  wg_barrier();
  const __amdgpu_buffer_rsrc_t rs = __builtin_amdgcn_make_buffer_rsrc((void*)(ws + (size_t)cp * WS_COPY + (size_t)nh * 64 * PC_SIZE), 0, 64 * PC_SIZE, 0x00020000);
  const unsigned l16 = lane * 16;
  char* stg = img + wv * 4096;
  if (wv >= 4) {
    const u16* xb = x + (size_t)(mt * 64 + (wv - 4) * 16) * Ktot;
#pragma unroll 4
    for (int i = 0; i < 16; ++i) {
      const u16* src = xb + (size_t)i * Ktot + ((lane ^ i) << 3);
      char* dst = smem + ((wv - 4) * 16 + i) * 1024;
      __builtin_amdgcn_global_load_lds((const unsigned*)src, (unsigned*)dst, 16, 0, 2);
      __builtin_amdgcn_global_load_lds((const unsigned*)(src + 512), (unsigned*)(dst + 65536), 16, 0, 2);
    }
  }
  u64 ready = 0;
  int todo = rank;
  unsigned round = 0;
  for (;;) {
    round += 4;
    if (wv < 4) {
      const int kq = todo & 3;
      const int pdst = todo * PC_SIZE;
      const int sw = (gp >> 2) & 1, h = (gp >> 1) & 1, jp = gp & 1;
      const int rho0 = 8 * (o & 3) + 4 * (o >> 2);
      const int fl0 = rho0 + 32 * h;
      unsigned emask = 0;
      unsigned V0[2][2], V1[2][2], NI[2][2];
#pragma unroll
      for (int g = 0; g < 2; ++g)
#pragma unroll
        for (int pp = 0; pp < 2; ++pp) {
          const unsigned a = r[g * 4 + 0][pp], b = r[g * 4 + 1][pp], c = r[g * 4 + 2][pp], d = r[g * 4 + 3][pp];
          const unsigned ma = a & 0x7FFF7FFFu, mb = b & 0x7FFF7FFFu, mc = c & 0x7FFF7FFFu, md = d & 0x7FFF7FFFu;
          const unsigned h01 = pk_max(ma, mb), l01 = pk_min(ma, mb), h23 = pk_max(mc, md), l23 = pk_min(mc, md);
          unsigned thr = pk_max(pk_min(h01, h23), pk_max(l01, l23));
          thr = pk_max(thr, 0x00010001u);
          const unsigned na = pk_ltmask(ma, thr), nb = pk_ltmask(mb, thr), nc = pk_ltmask(mc, thr), nd = pk_ltmask(md, thr);
          const unsigned v0 = bfi(na, bfi(nb, c, b), a);
          unsigned v1 = bfi(nd, bfi(nc, b, c), d);
          v1 &= ~(na & nd & (nb ^ nc));
          const unsigned p0 = na & bfi(nb, 0x00020002u, 0x00010001u);
          const unsigned p1 = bfi(nd, bfi(nc, 0x00040004u, 0x00080008u), 0x000C000Cu);
          V0[g][pp] = v0; V1[g][pp] = v1; NI[g][pp] = p0 | p1;
          const unsigned eb = ~nb & ~na & ~(nc & nd), ec = ~nc & ~nd & ~(na & nb);
          const unsigned e2 = (eb & 0x00020001u) | (ec & 0x00080004u);
          emask |= ((e2 | (e2 >> 16)) & 15u) << (g * 8 + pp * 4);
        }
      char* fp = stg + sw * 1024 + fl0 * 16 + jp * 8;
#pragma unroll
      for (int pp = 0; pp < 2; ++pp) {
        u32x2 lo, hi;
        lo.x = __builtin_amdgcn_perm(V1[0][pp], V0[0][pp], 0x05040100u); lo.y = __builtin_amdgcn_perm(V1[1][pp], V0[1][pp], 0x05040100u);
        hi.x = __builtin_amdgcn_perm(V1[0][pp], V0[0][pp], 0x07060302u); hi.y = __builtin_amdgcn_perm(V1[1][pp], V0[1][pp], 0x07060302u);
        *(u32x2*)(fp + pp * 32) = lo; *(u32x2*)(fp + pp * 32 + 16) = hi;
        const unsigned by = NI[0][pp] | (NI[1][pp] << 4);
        char* q = stg + 2048 + (fl0 + 2 * pp) * 4 + sw * 2 + jp;
        *(unsigned char*)q = (unsigned char)by; *(unsigned char*)(q + 4) = (unsigned char)(by >> 16);
      }
      int tot = 0;
      for (unsigned rem = emask;;) { const u64 bl = __ballot(rem != 0); if (!bl) break; tot += __popcll(bl); rem &= rem - 1; }
      if (lane == 0) { *(int*)(img + PL_WT + wv * 4) = tot;
        asm volatile("s_waitcnt lgkmcnt(0)" ::: "memory");
        __hip_atomic_fetch_add((unsigned*)(img + PL_CT), 1u, __ATOMIC_RELAXED, __HIP_MEMORY_SCOPE_WORKGROUP); }
      __builtin_amdgcn_raw_buffer_store_b128(*(const u32x4*)(stg + l16), rs, PC_VAL + wv * 2048 + l16, pdst, SC1);
      __builtin_amdgcn_raw_buffer_store_b128(*(const u32x4*)(stg + 1024 + l16), rs, PC_VAL + wv * 2048 + 1024 + l16, pdst, SC1);
      __builtin_amdgcn_raw_buffer_store_b32(*(const unsigned*)(stg + 2048 + lane * 4), rs, PC_IDX + wv * 256 + lane * 4, pdst, SC1);
      STAMP(1);
      while (__hip_atomic_load((unsigned*)(img + PL_CT), __ATOMIC_RELAXED, __HIP_MEMORY_SCOPE_WORKGROUP) < round) {}
      asm volatile("" ::: "memory");
      const u32x4 wt = *(const u32x4*)(img + PL_WT);
      const int total = (int)(wt.x + wt.y + wt.z + wt.w);
      if (wv == 0 && lane >= total && lane < 16) __builtin_amdgcn_raw_buffer_store_b32(0u, rs, PC_TR + lane * 4, pdst, SC1);
      unsigned s2mask = 0;
      int base = (wv > 0 ? (int)wt.x : 0) + (wv > 1 ? (int)wt.y : 0) + (wv > 2 ? (int)wt.z : 0);
      for (unsigned rem = emask;;) {
        const u64 bl = __ballot(rem != 0); if (!bl) break;
        if (rem != 0) {
          const int q = base + __builtin_amdgcn_mbcnt_hi((unsigned)(bl >> 32), __builtin_amdgcn_mbcnt_lo((unsigned)bl, 0));
          const int bit = __builtin_ctz(rem);
          if (q < 16) {
            const int g = bit >> 3, pp = (bit >> 2) & 1, e = (bit >> 1) & 1, hf = bit & 1;
            unsigned dv = 0;
#pragma unroll
            for (int gg = 0; gg < 2; ++gg)
#pragma unroll
              for (int ee = 0; ee < 2; ++ee)
#pragma unroll
                for (int p2 = 0; p2 < 2; ++p2) dv = (g == gg && e == ee && pp == p2) ? r[gg * 4 + 1 + ee][p2] : dv;
            const unsigned val = hf ? (dv >> 16) : (dv & 0xFFFFu);
            const unsigned gi = (unsigned)((kq & 1) * 64 + gp * 2 + g);
            const unsigned rho = (unsigned)(rho0 + 2 * pp + hf);
            __builtin_amdgcn_raw_buffer_store_b32((val << 16) | ((unsigned)e << 15) | (rho << 10) | (gi << 3), rs, PC_TR + q * 4, pdst, SC1);
          } else s2mask |= 1u << bit;
        }
        base += __popcll(bl); rem &= rem - 1;
      }
      unsigned stepm = 0;
      if (total > 16) {
        const u64 bl = __ballot(s2mask != 0);
        stepm = ((unsigned)bl ? 1u : 0u) | ((unsigned)(bl >> 32) ? 2u : 0u);
#pragma unroll
        for (int pp = 0; pp < 2; ++pp) {
          unsigned wv2[2][2];
#pragma unroll
          for (int g = 0; g < 2; ++g) {
            const unsigned bb = r[g * 4 + 1][pp], cc = r[g * 4 + 2][pp];
            const unsigned bits = (s2mask >> (g * 8 + pp * 4)) & 15u;
            wv2[0][g] = ((bits & 1u) ? (bb & 0xFFFFu) : 0u) | ((bits & 4u) ? (cc << 16) : 0u);
            wv2[1][g] = ((bits & 2u) ? (bb >> 16) : 0u) | ((bits & 8u) ? (cc & 0xFFFF0000u) : 0u);
          }
          *(u32x2*)(fp + pp * 32) = (u32x2){wv2[0][0], wv2[0][1]}; *(u32x2*)(fp + pp * 32 + 16) = (u32x2){wv2[1][0], wv2[1][1]};
        }
        __builtin_amdgcn_raw_buffer_store_b128(*(const u32x4*)(stg + l16), rs, PC_S2V + wv * 2048 + l16, pdst, SC1);
        __builtin_amdgcn_raw_buffer_store_b128(*(const u32x4*)(stg + 1024 + l16), rs, PC_S2V + wv * 2048 + 1024 + l16, pdst, SC1);
      }
      if (lane == 0) __builtin_amdgcn_raw_buffer_store_b8((unsigned char)stepm, rs, PC_SM + wv, pdst, SC1);
      asm volatile("s_waitcnt vmcnt(0)" : "+v"(ep) :: "memory");
      STAMP(2);
      const unsigned t0 = ep + 1u;
      const u64 tag = ((u64)(t0 * 0x9E3779B1u ^ 0x7F4A7C15u) << 32) | t0;
      if (lane == 0) {
        const unsigned dn = __hip_atomic_fetch_add((unsigned*)(img + PL_DN), 1u, __ATOMIC_RELAXED, __HIP_MEMORY_SCOPE_WORKGROUP);
        if (dn + 1u == round) __hip_atomic_store(flags + todo, tag, __ATOMIC_RELAXED, __HIP_MEMORY_SCOPE_AGENT);
      }
      if (wv == 0) {
        if (round > 4) ready |= 1ull << todo;
#pragma unroll 1
        for (int spin = 0; spin < 4096; ++spin) {
          const u64 f = __hip_atomic_load(flags + lane, __ATOMIC_RELAXED, __HIP_MEMORY_SCOPE_AGENT);
          ready |= __ballot(f == tag);
          if (ready == ~0ull) break;
          __builtin_amdgcn_s_sleep(1);
        }
        ready |= 1ull << todo;
        const int nx = ready == ~0ull ? -1 : __builtin_ctzll(~ready);
        if (lane < 8) *(int*)(img + lane * 4096 + 4092) = nx;
        if (nx < 0 && lane == 0) __hip_atomic_fetch_add(epoch, 1u, __ATOMIC_RELAXED, __HIP_MEMORY_SCOPE_AGENT);
      }
    } else {
      STAMP(1);
      asm volatile("s_waitcnt vmcnt(0)" ::: "memory");
      STAMP(2);
    }
    wg_barrier();
    todo = __builtin_amdgcn_readfirstlane(*(const int*)(stg + 4092));
    if (todo < 0) break;
    if (wv < 4) {
      const u16* src = wsrc + (size_t)((todo & 3) * 256) * Ntot + (todo >> 2) * 32;
#pragma unroll
      for (int i = 0; i < 8; ++i) r[i] = __builtin_nontemporal_load((const u32x2*)(src + (size_t)i * Ntot));
    }
    wg_barrier();
  }
  __builtin_amdgcn_fence(__ATOMIC_ACQUIRE, "wavefront");
  STAMP(3);

  const int pb0 = wv * 8 * PC_SIZE;
  const bool late = wv >= 4;
  f32x16 acc[2][2];
#pragma unroll
  for (int nb = 0; nb < 2; ++nb) {
    const u32x4 b0 = bq[nb][0], b1 = bq[nb][1];
    const unsigned bw[8] = {b0.x, b0.y, b0.z, b0.w, b1.x, b1.y, b1.z, b1.w};
#pragma unroll
    for (int i = 0; i < 8; ++i) { acc[nb][0][2 * i] = __uint_as_float(bw[i] << 16); acc[nb][0][2 * i + 1] = __uint_as_float(bw[i] & 0xFFFF0000u); }
    acc[nb][1] = acc[nb][0];
  }
  const unsigned rowb = ml * 1024;
  const unsigned u = (unsigned)(hb ^ (ml & 15));

  auto tie_phase = [&]() __attribute__((always_inline)) {
      unsigned rec[2];
      rec[0] = LDI((lane >> 4) * PC_SIZE + PC_TR + (lane & 15) * 4, pb0);
      rec[1] = LDI((lane >> 4) * PC_SIZE + PC_TR + (lane & 15) * 4, pb0 + 4 * PC_SIZE);
      const unsigned smk = LDI((lane & 7) * PC_SIZE + PC_SM, pb0);
      *(u32x4*)(stg + l16) = (u32x4){0, 0, 0, 0};
      *(u32x4*)(stg + 1024 + l16) = (u32x4){0, 0, 0, 0};
      {
        const unsigned tpos = 2048 + ((lane >> 4) * 16 + ((lane >> 2) & 1) * 8 + ((lane >> 3) & 1) * 4 + (lane & 3)) * 4;
        *(unsigned*)(stg + tpos) = rec[0] & 0x3F8u;
        *(unsigned*)(stg + tpos + 256) = rec[1] & 0x3F8u;
      }
      const unsigned apos = (unsigned)(((lane >> 4) & 1) * 1024 + ((lane >> 3) & 1) * 512 + (lane & 7) * 2);
      const unsigned tb = rowb + (unsigned)((ml & 15) << 4);
      u64 cls[2]; cls[0] = __ballot((rec[0] >> 15) & 1u); cls[1] = __ballot((rec[1] >> 15) & 1u);
      unsigned tbm[2][2];
      tbm[0][0] = tb; tbm[0][1] = tb + 32768u; tbm[1][0] = tb + 65536u; tbm[1][1] = tb + 98304u;
      asm("" : "+v"(tbm[0][1])); asm("" : "+v"(tbm[1][0])); asm("" : "+v"(tbm[1][1]));
      u32x2 g[2][8]; u32x4 at[2]; u32x8 ko;
      { const u32x4 k0 = *(const u32x4*)(stg + 2048 + hb * 32), k1 = *(const u32x4*)(stg + 2048 + hb * 32 + 16);
        ko = (u32x8){k0.x, k0.y, k0.z, k0.w, k1.x, k1.y, k1.z, k1.w}; }
#pragma unroll
      for (int un = 0; un <= 16; ++un) {
        SB;
        if (un < 16) {
          const int pc = un >> 1, mb = un & 1, nb = pc >> 2, bch = (pc >> 1) & 1;
          u16* ap = (u16*)(stg + apos + ((rec[nb] >> 10) & 31u) * 16);
          if ((un & 3) == 0 && hb == bch) *ap = (u16)(rec[nb] >> 16);
#pragma unroll
          for (int j = 0; j < 8; ++j) g[un & 1][j] = *(const u32x2*)(smem + (tbm[bch][mb] ^ ko[j]));
          if (mb == 0) at[pc & 1] = *(const u32x4*)(stg + (pc & 1) * 1024 + l16);
          if ((un & 3) == 2 && hb == bch) *ap = (u16)0;
          if (mb == 1 && pc < 7) {
            const u32x4 k0 = *(const u32x4*)(stg + 2048 + (pc + 1) * 64 + hb * 32), k1 = *(const u32x4*)(stg + 2048 + (pc + 1) * 64 + hb * 32 + 16);
            ko = (u32x8){k0.x, k0.y, k0.z, k0.w, k1.x, k1.y, k1.z, k1.w};
          }
        }
        SB;
        if (un > 0) {
          const int up = un - 1, pp = up >> 1, mb = up & 1, nb = pp >> 2;
          const unsigned c16 = (unsigned)(cls[nb] >> (16 * (pp & 3))) & 0xFFFFu;
          u64 rep; asm("s_bitreplicate_b64_b32 %0, %1" : "=s"(rep) : "s"(c16));
          const unsigned iw = ((unsigned)rep & 0x55555555u) | 0x88888888u;
          const unsigned ti = iw >> (16 * hb);
          u32x8 bt;
#pragma unroll
          for (int j = 0; j < 8; ++j) bt[j] = __builtin_amdgcn_alignbit(g[up & 1][j].y, g[up & 1][j].x, 16);
          acc[nb][mb] = SMF(at[pp & 1], bt, acc[nb][mb], ti, 0);
        }
      }
      SB;
      if (__ballot(smk != 0u)) {
#pragma unroll
        for (int nb = 0; nb < 2; ++nb) {
#pragma unroll 1
          for (int kq = 0; kq < 4; ++kq) {
            unsigned sm = (unsigned)__builtin_amdgcn_readlane((int)smk, nb * 4 + kq);
            while (sm) {
              const int bit = __builtin_ctz(sm); sm &= sm - 1;
              const int st = (bit >> 3) * 2 + (bit & 1);
              const u32x4 a2 = LDW(l16 + PC_S2V, pb0 + (nb * 4 + kq) * PC_SIZE + st * 1024);
              const unsigned c0 = (unsigned)(4 * (st & 3));
              const unsigned xb = rowb + (unsigned)((kq >> 1) * 65536 + (kq & 1) * 512 + (st >> 2) * 256);
              const unsigned a0 = xb + ((c0 ^ u) << 4), a1 = xb + (((c0 + 2) ^ u) << 4);
#pragma unroll
              for (int mb = 0; mb < 2; ++mb) {
                const u32x8 b = ldB(smem, a0, a1, mb * 32768);
                acc[nb][mb] = SMF(a2, b, acc[nb][mb], 0x99999999u, 0);
              }
            }
          }
        }
      }
  };
  if (late) tie_phase();
  STAMP(4);
  {
      __builtin_amdgcn_s_setprio(1);
      const int kq0 = (mt >> 1) & 3;
      const unsigned l4 = lane * 4 + PC_IDX;
      u32x4 a[8][2]; unsigned ix[4][2];
      {
        const int pb = pb0 + kq0 * PC_SIZE;
#pragma unroll
        for (int s = 0; s < DEPTH; ++s) {
          if ((s & 1) == 0) { ix[s >> 1][0] = LDI(l4 + (s >> 1) * 256, pb); ix[s >> 1][1] = LDI(l4 + (s >> 1) * 256, pb + 4 * PC_SIZE); }
#pragma unroll
          for (int nb = 0; nb < 2; ++nb) a[s][nb] = LDW(l16 + s * 1024, pb + nb * 4 * PC_SIZE);
        }
      }
      u32x8 bc[2];
      {
        const unsigned xb = rowb + (unsigned)((kq0 >> 1) * 65536 + (kq0 & 1) * 512);
        const unsigned a0 = xb + (u << 4), a1 = xb + ((2u ^ u) << 4);
        bc[0] = ldB(smem, a0, a1, 0); bc[1] = ldB(smem, a0, a1, 32768);
      }
#define MM(mb, nb) do { if (s & 1) acc[nb][mb] = SMF(a[s][nb], bc[mb], acc[nb][mb], ix[s >> 1][nb], 1); else acc[nb][mb] = SMF(a[s][nb], bc[mb], acc[nb][mb], ix[s >> 1][nb], 0); } while (0)
#define STEP(LAST) \
      _Pragma("unroll") for (int s = 0; s < 8; ++s) { \
        const int sp = s + DEPTH, sn = (s + 1) & 7, pq = sp < 8 ? pb : pbn; \
        const bool pf = sp < 8 || !(LAST); \
        const unsigned a0 = s < 7 ? xa[2 * (sn & 3)] : xn0, a1 = s < 7 ? xa[2 * (sn & 3) + 1] : xn1; \
        u32x8 bn[2]; u32x4 an[2]; unsigned in[2]; \
        SB; \
        MM(0, 0); \
        if (s < 7 || !(LAST)) bn[0] = ldB(smem, a0, a1, (sn >> 2) * 256); \
        SB; \
        MM(0, 1); \
        if (s < 7 || !(LAST)) bn[1] = ldB(smem, a0, a1, (sn >> 2) * 256 + 32768); \
        SB; \
        MM(1, 0); \
        if (pf && (sp & 1) == 0) { in[0] = LDI(l4 + ((sp & 7) >> 1) * 256, pq); in[1] = LDI(l4 + ((sp & 7) >> 1) * 256, pq + 4 * PC_SIZE); } \
        if (pf) an[0] = LDW(l16 + (sp & 7) * 1024, pq); \
        SB; \
        MM(1, 1); \
        if (pf) an[1] = LDW(l16 + (sp & 7) * 1024, pq + 4 * PC_SIZE); \
        if (pf) { a[sp & 7][0] = an[0]; a[sp & 7][1] = an[1]; } \
        if (pf && (sp & 1) == 0) { ix[(sp & 7) >> 1][0] = in[0]; ix[(sp & 7) >> 1][1] = in[1]; } \
        if (s < 7 || !(LAST)) { bc[0] = bn[0]; bc[1] = bn[1]; } \
      }
#define ITER_SETUP \
        const unsigned xb = rowb + (unsigned)((kq >> 1) * 65536 + (kq & 1) * 512); \
        const unsigned xbn = rowb + (unsigned)((kqn >> 1) * 65536 + (kqn & 1) * 512); \
        unsigned xa[8]; _Pragma("unroll") for (int c = 0; c < 8; ++c) xa[c] = xb + (((unsigned)(2 * c) ^ u) << 4); \
        const unsigned xn0 = xbn + (u << 4), xn1 = xbn + ((2u ^ u) << 4);
#pragma unroll 1
      for (int it = 0; it < 3; ++it) {
        const int kq = (kq0 + it) & 3, kqn = (kq + 1) & 3;
        const int pb = pb0 + kq * PC_SIZE, pbn = pb0 + kqn * PC_SIZE;
        ITER_SETUP
        STEP(false)
      }
      {
        const int kq = (kq0 + 3) & 3, kqn = kq;
        const int pb = pb0 + kq * PC_SIZE, pbn = pb;
        ITER_SETUP
        STEP(true)
      }
      SB;
      __builtin_amdgcn_s_setprio(0);
  }
  STAMP(5);
  if (!late) tie_phase();
  {
    u16* ob = out + (size_t)(mt * 64) * Ntot + NB0 * 32;
#pragma unroll
    for (int mb = 0; mb < 2; ++mb) {
#pragma unroll
      for (int nb = 0; nb < 2; ++nb)
#pragma unroll
        for (int hf = 0; hf < 2; ++hf) {
          u32x4 v;
#pragma unroll
          for (int i = 0; i < 4; ++i) {
            const f32x2 f = {acc[nb][mb][hf * 8 + 2 * i], acc[nb][mb][hf * 8 + 2 * i + 1]};
            v[i] = __builtin_bit_cast(unsigned, __builtin_convertvector(f, bf16x2));
          }
          const int ch = nb * 4 + hb * 2 + hf;
          *(u32x4*)(stg + ml * 128 + ((ch ^ (ml & 7)) << 4)) = v;
        }
#pragma unroll
      for (int i = 0; i < 4; ++i) {
        const int row = (lane >> 3) + 8 * i;
        const u32x4 v = *(const u32x4*)(stg + row * 128 + (((lane & 7) ^ (row & 7)) << 4));
        __builtin_nontemporal_store(v, (u32x4*)(ob + (size_t)(mb * 32 + row) * Ntot + (lane & 7) * 8));
      }
    }
  }
  STAMP(6);
  asm volatile("s_waitcnt vmcnt(0)" ::: "memory");
  STAMP(7);
  STAMP_FLUSH(8);
}

extern "C" void kernel_launch(void* const* d_in, const int* in_sizes, int n_in, void* d_out, int out_size, void* d_ws, size_t ws_size, hipStream_t stream) {
  (void)hipFuncSetAttribute((const void*)sp24_gemm, hipFuncAttributeMaxDynamicSharedMemorySize, 163840);
  sp24_gemm<<<256, 512, 163840, stream>>>((const u16*)d_in[0], (const u16*)d_in[1], (const u16*)d_in[2], (u16*)d_out, (char*)d_ws);
}
```

```cpp
#define STAUX 2
#define XAUX 0
#include <hip/hip_runtime.h>
typedef unsigned short u16;
typedef unsigned long long u64;
typedef __bf16 bf16x8 __attribute__((ext_vector_type(8)));
typedef __bf16 bf16x16 __attribute__((ext_vector_type(16)));
typedef __bf16 bf16x2 __attribute__((ext_vector_type(2)));
typedef float f32x2 __attribute__((ext_vector_type(2)));
typedef float f32x16 __attribute__((ext_vector_type(16)));
typedef unsigned u32x2 __attribute__((ext_vector_type(2)));
typedef unsigned u32x4 __attribute__((ext_vector_type(4)));
typedef unsigned u32x8 __attribute__((ext_vector_type(8)));
typedef unsigned short u16x2 __attribute__((ext_vector_type(2)));
typedef short s16x2 __attribute__((ext_vector_type(2)));
typedef __attribute__((address_space(1))) unsigned gu32;
typedef __attribute__((address_space(1))) u64 gu64;

constexpr int Ntot = 1024, Ktot = 1024;
constexpr int PC_VAL = 0  , PC_IDX = 8192  , PC_TR = 9216  , PC_SM = 9280  ;
constexpr int PC_S2V = 10240, PC_SIZE = 18432;
constexpr int PL_WT = 16384, PL_CT = 16400, PL_DN = 16404;
constexpr int WS_COPY = 128 * PC_SIZE;
constexpr int WS_FLAG = 2 * WS_COPY, WS_EPOCH = WS_FLAG + 4096;
constexpr int SC1 = 16;
#define DEPTH_ 7
constexpr int DEPTH = DEPTH_;

#define STAMP(i)
#define STAMP_FLUSH(n)

__device__ __forceinline__ unsigned pk_max(unsigned a, unsigned b) { return __builtin_bit_cast(unsigned, __builtin_elementwise_max(__builtin_bit_cast(u16x2, a), __builtin_bit_cast(u16x2, b))); }
__device__ __forceinline__ unsigned pk_min(unsigned a, unsigned b) { return __builtin_bit_cast(unsigned, __builtin_elementwise_min(__builtin_bit_cast(u16x2, a), __builtin_bit_cast(u16x2, b))); }
__device__ __forceinline__ unsigned pk_ltmask(unsigned a, unsigned b) {
  s16x2 d = __builtin_bit_cast(s16x2, a) - __builtin_bit_cast(s16x2, b); d = d >> (s16x2){15, 15}; return __builtin_bit_cast(unsigned, d); }
__device__ __forceinline__ unsigned bfi(unsigned m, unsigned x, unsigned y) { return (m & x) | (~m & y); }
__device__ __forceinline__ void wg_barrier() {
  asm volatile("s_waitcnt lgkmcnt(0)" ::: "memory"); __builtin_amdgcn_s_barrier(); asm volatile("" ::: "memory"); }

#define SB __builtin_amdgcn_sched_barrier(0)
#define SMF(A, B, C, I, AB) __builtin_amdgcn_smfmac_f32_32x32x32_bf16(__builtin_bit_cast(bf16x8, A), __builtin_bit_cast(bf16x16, B), C, (int)(I), 0, AB)
#define LDW(off, soff) __builtin_amdgcn_raw_buffer_load_b128(rs, off, soff, SC1)
#define LDI(off, soff) __builtin_amdgcn_raw_buffer_load_b32(rs, off, soff, SC1)

__device__ __forceinline__ u32x8 ldB(const char* smem, unsigned a0, unsigned a1, int off) {
  const u32x4 lo = *(const u32x4*)(smem + a0 + off), hi = *(const u32x4*)(smem + a1 + off);
  return (u32x8){lo.x, lo.y, lo.z, lo.w, hi.x, hi.y, hi.z, hi.w};
}

__global__ __launch_bounds__(512, 2) void sp24_gemm(const u16* __restrict__ x, const u16* __restrict__ w, const u16* __restrict__ bias,
                                                    u16* __restrict__ out, char* ws) {
  extern __shared__ __attribute__((aligned(16))) char smem[];
  const int bid = blockIdx.x, xcd = bid & 7, jj = bid >> 3;
  const int mt = xcd * 16 + (jj >> 1), nh = jj & 1, cp = xcd >> 2;
  const int grp = cp * 2 + nh, rank = (xcd & 3) * 16 + (jj >> 1);
  const int tid = threadIdx.x, lane = tid & 63;
  const int wv = __builtin_amdgcn_readfirstlane(tid >> 6);
  STAMP(0);
  const int gp = (tid >> 3) & 31, o = tid & 7;
  const u16* wsrc = w + (size_t)(gp * 8) * Ntot + nh * 512 + o * 4;
  u32x2 r[8];
  if (wv < 4) {
    const u16* src = wsrc + (size_t)((rank & 3) * 256) * Ntot + (rank >> 2) * 32;
#pragma unroll
    for (int i = 0; i < 8; ++i) r[i] = __builtin_nontemporal_load((const u32x2*)(src + (size_t)i * Ntot));
  }
  SB;
  char* img = smem + 131072;
  if (tid == 0) *(u32x2*)(img + PL_CT) = (u32x2){0u, 0u};
  const int ml = lane & 31, hb = lane >> 5;
  gu64* flags = (gu64*)(ws + WS_FLAG + grp * 512);
  gu32* epoch = (gu32*)(ws + WS_EPOCH + grp * 256);
  unsigned ep = __hip_atomic_load(epoch, __ATOMIC_RELAXED, __HIP_MEMORY_SCOPE_AGENT);
  const int NB0 = nh * 16 + wv * 2;
  u32x4 bq[2][2];
#pragma unroll
  for (int nb = 0; nb < 2; ++nb) { const u32x4* bp = (const u32x4*)(bias + (NB0 + nb) * 32 + hb * 16); bq[nb][0] = bp[0]; bq[nb][1] = bp[1]; }
  wg_barrier();
  const __amdgpu_buffer_rsrc_t rs = __builtin_amdgcn_make_buffer_rsrc((void*)(ws + (size_t)cp * WS_COPY + (size_t)nh * 64 * PC_SIZE), 0, 64 * PC_SIZE, 0x00020000);
  const unsigned l16 = lane * 16;
  char* stg = img + wv * 4096;
  if (wv >= 4) {
    const u16* xb = x + (size_t)(mt * 64 + (wv - 4) * 16) * Ktot;
#pragma unroll 4
    for (int i = 0; i < 16; ++i) {
      const u16* src = xb + (size_t)i * Ktot + ((lane ^ i) << 3);
      char* dst = smem + ((wv - 4) * 16 + i) * 1024;
      __builtin_amdgcn_global_load_lds((const unsigned*)src, (unsigned*)dst, 16, 0, XAUX);
      __builtin_amdgcn_global_load_lds((const unsigned*)(src + 512), (unsigned*)(dst + 65536), 16, 0, XAUX);
    }
  }
  u64 ready = 0;
  int todo = rank;
  unsigned round = 0;
  for (;;) {
    round += 4;
    if (wv < 4) {
      const int kq = todo & 3;
      const int pdst = todo * PC_SIZE;
      const int sw = (gp >> 2) & 1, h = (gp >> 1) & 1, jp = gp & 1;
      const int rho0 = 8 * (o & 3) + 4 * (o >> 2);
      const int fl0 = rho0 + 32 * h;
      unsigned emask = 0;
      unsigned V0[2][2], V1[2][2], NI[2][2];
#pragma unroll
      for (int g = 0; g < 2; ++g)
#pragma unroll
        for (int pp = 0; pp < 2; ++pp) {
          const unsigned a = r[g * 4 + 0][pp], b = r[g * 4 + 1][pp], c = r[g * 4 + 2][pp], d = r[g * 4 + 3][pp];
          const unsigned ma = a & 0x7FFF7FFFu, mb = b & 0x7FFF7FFFu, mc = c & 0x7FFF7FFFu, md = d & 0x7FFF7FFFu;
          const unsigned h01 = pk_max(ma, mb), l01 = pk_min(ma, mb), h23 = pk_max(mc, md), l23 = pk_min(mc, md);
          unsigned thr = pk_max(pk_min(h01, h23), pk_max(l01, l23));
          thr = pk_max(thr, 0x00010001u);
          const unsigned na = pk_ltmask(ma, thr), nb = pk_ltmask(mb, thr), nc = pk_ltmask(mc, thr), nd = pk_ltmask(md, thr);
          const unsigned v0 = bfi(na, bfi(nb, c, b), a);
          unsigned v1 = bfi(nd, bfi(nc, b, c), d);
          v1 &= ~(na & nd & (nb ^ nc));
          const unsigned p0 = na & bfi(nb, 0x00020002u, 0x00010001u);
          const unsigned p1 = bfi(nd, bfi(nc, 0x00040004u, 0x00080008u), 0x000C000Cu);
          V0[g][pp] = v0; V1[g][pp] = v1; NI[g][pp] = p0 | p1;
          const unsigned eb = ~nb & ~na & ~(nc & nd), ec = ~nc & ~nd & ~(na & nb);
          const unsigned e2 = (eb & 0x00020001u) | (ec & 0x00080004u);
          emask |= ((e2 | (e2 >> 16)) & 15u) << (g * 8 + pp * 4);
        }
      char* fp = stg + sw * 1024 + fl0 * 16 + jp * 8;
#pragma unroll
      for (int pp = 0; pp < 2; ++pp) {
        u32x2 lo, hi;
        lo.x = __builtin_amdgcn_perm(V1[0][pp], V0[0][pp], 0x05040100u); lo.y = __builtin_amdgcn_perm(V1[1][pp], V0[1][pp], 0x05040100u);
        hi.x = __builtin_amdgcn_perm(V1[0][pp], V0[0][pp], 0x07060302u); hi.y = __builtin_amdgcn_perm(V1[1][pp], V0[1][pp], 0x07060302u);
        *(u32x2*)(fp + pp * 32) = lo; *(u32x2*)(fp + pp * 32 + 16) = hi;
        const unsigned by = NI[0][pp] | (NI[1][pp] << 4);
        char* q = stg + 2048 + (fl0 + 2 * pp) * 4 + sw * 2 + jp;
        *(unsigned char*)q = (unsigned char)by; *(unsigned char*)(q + 4) = (unsigned char)(by >> 16);
      }
      int tot = 0;
      for (unsigned rem = emask;;) { const u64 bl = __ballot(rem != 0); if (!bl) break; tot += __popcll(bl); rem &= rem - 1; }
      if (lane == 0) { *(int*)(img + PL_WT + wv * 4) = tot;
        asm volatile("s_waitcnt lgkmcnt(0)" ::: "memory");
        __hip_atomic_fetch_add((unsigned*)(img + PL_CT), 1u, __ATOMIC_RELAXED, __HIP_MEMORY_SCOPE_WORKGROUP); }
      __builtin_amdgcn_raw_buffer_store_b128(*(const u32x4*)(stg + l16), rs, PC_VAL + wv * 2048 + l16, pdst, SC1);
      __builtin_amdgcn_raw_buffer_store_b128(*(const u32x4*)(stg + 1024 + l16), rs, PC_VAL + wv * 2048 + 1024 + l16, pdst, SC1);
      __builtin_amdgcn_raw_buffer_store_b32(*(const unsigned*)(stg + 2048 + lane * 4), rs, PC_IDX + wv * 256 + lane * 4, pdst, SC1);
      STAMP(1);
      while (__hip_atomic_load((unsigned*)(img + PL_CT), __ATOMIC_RELAXED, __HIP_MEMORY_SCOPE_WORKGROUP) < round) {}
      asm volatile("" ::: "memory");
      const u32x4 wt = *(const u32x4*)(img + PL_WT);
      const int total = (int)(wt.x + wt.y + wt.z + wt.w);
      if (wv == 0 && lane >= total && lane < 16) __builtin_amdgcn_raw_buffer_store_b32(0u, rs, PC_TR + lane * 4, pdst, SC1);
      unsigned s2mask = 0;
      int base = (wv > 0 ? (int)wt.x : 0) + (wv > 1 ? (int)wt.y : 0) + (wv > 2 ? (int)wt.z : 0);
      for (unsigned rem = emask;;) {
        const u64 bl = __ballot(rem != 0); if (!bl) break;
        if (rem != 0) {
          const int q = base + __builtin_amdgcn_mbcnt_hi((unsigned)(bl >> 32), __builtin_amdgcn_mbcnt_lo((unsigned)bl, 0));
          const int bit = __builtin_ctz(rem);
          if (q < 16) {
            const int g = bit >> 3, pp = (bit >> 2) & 1, e = (bit >> 1) & 1, hf = bit & 1;
            unsigned dv = 0;
#pragma unroll
            for (int gg = 0; gg < 2; ++gg)
#pragma unroll
              for (int ee = 0; ee < 2; ++ee)
#pragma unroll
                for (int p2 = 0; p2 < 2; ++p2) dv = (g == gg && e == ee && pp == p2) ? r[gg * 4 + 1 + ee][p2] : dv;
            const unsigned val = hf ? (dv >> 16) : (dv & 0xFFFFu);
            const unsigned gi = (unsigned)((kq & 1) * 64 + gp * 2 + g);
            const unsigned rho = (unsigned)(rho0 + 2 * pp + hf);
            __builtin_amdgcn_raw_buffer_store_b32((val << 16) | ((unsigned)e << 15) | (rho << 10) | (gi << 3), rs, PC_TR + q * 4, pdst, SC1);
          } else s2mask |= 1u << bit;
        }
        base += __popcll(bl); rem &= rem - 1;
      }
      unsigned stepm = 0;
      if (total > 16) {
        const u64 bl = __ballot(s2mask != 0);
        stepm = ((unsigned)bl ? 1u : 0u) | ((unsigned)(bl >> 32) ? 2u : 0u);
#pragma unroll
        for (int pp = 0; pp < 2; ++pp) {
          unsigned wv2[2][2];
#pragma unroll
          for (int g = 0; g < 2; ++g) {
            const unsigned bb = r[g * 4 + 1][pp], cc = r[g * 4 + 2][pp];
            const unsigned bits = (s2mask >> (g * 8 + pp * 4)) & 15u;
            wv2[0][g] = ((bits & 1u) ? (bb & 0xFFFFu) : 0u) | ((bits & 4u) ? (cc << 16) : 0u);
            wv2[1][g] = ((bits & 2u) ? (bb >> 16) : 0u) | ((bits & 8u) ? (cc & 0xFFFF0000u) : 0u);
          }
          *(u32x2*)(fp + pp * 32) = (u32x2){wv2[0][0], wv2[0][1]}; *(u32x2*)(fp + pp * 32 + 16) = (u32x2){wv2[1][0], wv2[1][1]};
        }
        __builtin_amdgcn_raw_buffer_store_b128(*(const u32x4*)(stg + l16), rs, PC_S2V + wv * 2048 + l16, pdst, SC1);
        __builtin_amdgcn_raw_buffer_store_b128(*(const u32x4*)(stg + 1024 + l16), rs, PC_S2V + wv * 2048 + 1024 + l16, pdst, SC1);
      }
      if (lane == 0) __builtin_amdgcn_raw_buffer_store_b8((unsigned char)stepm, rs, PC_SM + wv, pdst, SC1);
      asm volatile("s_waitcnt vmcnt(0)" : "+v"(ep) :: "memory");
      STAMP(2);
      const unsigned t0 = ep + 1u;
      const u64 tag = ((u64)(t0 * 0x9E3779B1u ^ 0x7F4A7C15u) << 32) | t0;
      if (lane == 0) {
        const unsigned dn = __hip_atomic_fetch_add((unsigned*)(img + PL_DN), 1u, __ATOMIC_RELAXED, __HIP_MEMORY_SCOPE_WORKGROUP);
        if (dn + 1u == round) __hip_atomic_store(flags + todo, tag, __ATOMIC_RELAXED, __HIP_MEMORY_SCOPE_AGENT);
      }
      if (wv == 0) {
        if (round > 4) ready |= 1ull << todo;
#pragma unroll 1
        for (int spin = 0; spin < 4096; ++spin) {
          const u64 f = __hip_atomic_load(flags + lane, __ATOMIC_RELAXED, __HIP_MEMORY_SCOPE_AGENT);
          ready |= __ballot(f == tag);
          if (ready == ~0ull) break;
          __builtin_amdgcn_s_sleep(1);
        }
        ready |= 1ull << todo;
        const int nx = ready == ~0ull ? -1 : __builtin_ctzll(~ready);
        if (lane < 8) *(int*)(img + lane * 4096 + 4092) = nx;
        if (nx < 0 && lane == 0) __hip_atomic_fetch_add(epoch, 1u, __ATOMIC_RELAXED, __HIP_MEMORY_SCOPE_AGENT);
      }
    } else {
      STAMP(1);
      asm volatile("s_waitcnt vmcnt(0)" ::: "memory");
      STAMP(2);
    }
    wg_barrier();
    todo = __builtin_amdgcn_readfirstlane(*(const int*)(stg + 4092));
    if (todo < 0) break;
    if (wv < 4) {
      const u16* src = wsrc + (size_t)((todo & 3) * 256) * Ntot + (todo >> 2) * 32;
#pragma unroll
      for (int i = 0; i < 8; ++i) r[i] = __builtin_nontemporal_load((const u32x2*)(src + (size_t)i * Ntot));
    }
    wg_barrier();
  }
  __builtin_amdgcn_fence(__ATOMIC_ACQUIRE, "wavefront");
  STAMP(3);

  const int pb0 = wv * 8 * PC_SIZE;
  const bool late = wv >= 4;
  f32x16 acc[2][2];
#pragma unroll
  for (int nb = 0; nb < 2; ++nb) {
    const u32x4 b0 = bq[nb][0], b1 = bq[nb][1];
    const unsigned bw[8] = {b0.x, b0.y, b0.z, b0.w, b1.x, b1.y, b1.z, b1.w};
#pragma unroll
    for (int i = 0; i < 8; ++i) { acc[nb][0][2 * i] = __uint_as_float(bw[i] << 16); acc[nb][0][2 * i + 1] = __uint_as_float(bw[i] & 0xFFFF0000u); }
    acc[nb][1] = acc[nb][0];
  }
  const unsigned rowb = ml * 1024;
  const unsigned u = (unsigned)(hb ^ (ml & 15));

  auto tie_phase = [&]() __attribute__((always_inline)) {
      unsigned rec[2];
      rec[0] = LDI((lane >> 4) * PC_SIZE + PC_TR + (lane & 15) * 4, pb0);
      rec[1] = LDI((lane >> 4) * PC_SIZE + PC_TR + (lane & 15) * 4, pb0 + 4 * PC_SIZE);
      const unsigned smk = LDI((lane & 7) * PC_SIZE + PC_SM, pb0);
      *(u32x4*)(stg + l16) = (u32x4){0, 0, 0, 0};
      *(u32x4*)(stg + 1024 + l16) = (u32x4){0, 0, 0, 0};
      {
        const unsigned tpos = 2048 + ((lane >> 4) * 16 + ((lane >> 2) & 1) * 8 + ((lane >> 3) & 1) * 4 + (lane & 3)) * 4;
        *(unsigned*)(stg + tpos) = rec[0] & 0x3F8u;
        *(unsigned*)(stg + tpos + 256) = rec[1] & 0x3F8u;
      }
      const unsigned apos = (unsigned)(((lane >> 4) & 1) * 1024 + ((lane >> 3) & 1) * 512 + (lane & 7) * 2);
      const unsigned tb = rowb + (unsigned)((ml & 15) << 4);
      u64 cls[2]; cls[0] = __ballot((rec[0] >> 15) & 1u); cls[1] = __ballot((rec[1] >> 15) & 1u);
      unsigned tbm[2][2];
      tbm[0][0] = tb; tbm[0][1] = tb + 32768u; tbm[1][0] = tb + 65536u; tbm[1][1] = tb + 98304u;
      asm("" : "+v"(tbm[0][1])); asm("" : "+v"(tbm[1][0])); asm("" : "+v"(tbm[1][1]));
      u32x2 g[2][8]; u32x4 at[2]; u32x8 ko;
      { const u32x4 k0 = *(const u32x4*)(stg + 2048 + hb * 32), k1 = *(const u32x4*)(stg + 2048 + hb * 32 + 16);
        ko = (u32x8){k0.x, k0.y, k0.z, k0.w, k1.x, k1.y, k1.z, k1.w}; }
#pragma unroll
      for (int un = 0; un <= 16; ++un) {
        SB;
        if (un < 16) {
          const int pc = un >> 1, mb = un & 1, nb = pc >> 2, bch = (pc >> 1) & 1;
          u16* ap = (u16*)(stg + apos + ((rec[nb] >> 10) & 31u) * 16);
          if ((un & 3) == 0 && hb == bch) *ap = (u16)(rec[nb] >> 16);
#pragma unroll
          for (int j = 0; j < 8; ++j) g[un & 1][j] = *(const u32x2*)(smem + (tbm[bch][mb] ^ ko[j]));
          if (mb == 0) at[pc & 1] = *(const u32x4*)(stg + (pc & 1) * 1024 + l16);
          if ((un & 3) == 2 && hb == bch) *ap = (u16)0;
          if (mb == 1 && pc < 7) {
            const u32x4 k0 = *(const u32x4*)(stg + 2048 + (pc + 1) * 64 + hb * 32), k1 = *(const u32x4*)(stg + 2048 + (pc + 1) * 64 + hb * 32 + 16);
            ko = (u32x8){k0.x, k0.y, k0.z, k0.w, k1.x, k1.y, k1.z, k1.w};
          }
        }
        SB;
        if (un > 0) {
          const int up = un - 1, pp = up >> 1, mb = up & 1, nb = pp >> 2;
          const unsigned c16 = (unsigned)(cls[nb] >> (16 * (pp & 3))) & 0xFFFFu;
          u64 rep; asm("s_bitreplicate_b64_b32 %0, %1" : "=s"(rep) : "s"(c16));
          const unsigned iw = ((unsigned)rep & 0x55555555u) | 0x88888888u;
          const unsigned ti = iw >> (16 * hb);
          u32x8 bt;
#pragma unroll
          for (int j = 0; j < 8; ++j) bt[j] = __builtin_amdgcn_alignbit(g[up & 1][j].y, g[up & 1][j].x, 16);
          acc[nb][mb] = SMF(at[pp & 1], bt, acc[nb][mb], ti, 0);
        }
      }
      SB;
      if (__ballot(smk != 0u)) {
#pragma unroll
        for (int nb = 0; nb < 2; ++nb) {
#pragma unroll 1
          for (int kq = 0; kq < 4; ++kq) {
            unsigned sm = (unsigned)__builtin_amdgcn_readlane((int)smk, nb * 4 + kq);
            while (sm) {
              const int bit = __builtin_ctz(sm); sm &= sm - 1;
              const int st = (bit >> 3) * 2 + (bit & 1);
              const u32x4 a2 = LDW(l16 + PC_S2V, pb0 + (nb * 4 + kq) * PC_SIZE + st * 1024);
              const unsigned c0 = (unsigned)(4 * (st & 3));
              const unsigned xb = rowb + (unsigned)((kq >> 1) * 65536 + (kq & 1) * 512 + (st >> 2) * 256);
              const unsigned a0 = xb + ((c0 ^ u) << 4), a1 = xb + (((c0 + 2) ^ u) << 4);
#pragma unroll
              for (int mb = 0; mb < 2; ++mb) {
                const u32x8 b = ldB(smem, a0, a1, mb * 32768);
                acc[nb][mb] = SMF(a2, b, acc[nb][mb], 0x99999999u, 0);
              }
            }
          }
        }
      }
  };
  if (late) tie_phase();
  STAMP(4);
  {
      __builtin_amdgcn_s_setprio(1);
      const int kq0 = (mt >> 1) & 3;
      const unsigned l4 = lane * 4 + PC_IDX;
      u32x4 a[8][2]; unsigned ix[4][2];
      {
        const int pb = pb0 + kq0 * PC_SIZE;
#pragma unroll
        for (int s = 0; s < DEPTH; ++s) {
          if ((s & 1) == 0) { ix[s >> 1][0] = LDI(l4 + (s >> 1) * 256, pb); ix[s >> 1][1] = LDI(l4 + (s >> 1) * 256, pb + 4 * PC_SIZE); }
#pragma unroll
          for (int nb = 0; nb < 2; ++nb) a[s][nb] = LDW(l16 + s * 1024, pb + nb * 4 * PC_SIZE);
        }
      }
      u32x8 bc[2];
      {
        const unsigned xb = rowb + (unsigned)((kq0 >> 1) * 65536 + (kq0 & 1) * 512);
        const unsigned a0 = xb + (u << 4), a1 = xb + ((2u ^ u) << 4);
        bc[0] = ldB(smem, a0, a1, 0); bc[1] = ldB(smem, a0, a1, 32768);
      }
#define MM(mb, nb) do { if (s & 1) acc[nb][mb] = SMF(a[s][nb], bc[mb], acc[nb][mb], ix[s >> 1][nb], 1); else acc[nb][mb] = SMF(a[s][nb], bc[mb], acc[nb][mb], ix[s >> 1][nb], 0); } while (0)
#define STEP(LAST) \
      _Pragma("unroll") for (int s = 0; s < 8; ++s) { \
        const int sp = s + DEPTH, sn = (s + 1) & 7, pq = sp < 8 ? pb : pbn; \
        const bool pf = sp < 8 || !(LAST); \
        const unsigned a0 = s < 7 ? xa[2 * (sn & 3)] : xn0, a1 = s < 7 ? xa[2 * (sn & 3) + 1] : xn1; \
        u32x8 bn[2]; u32x4 an[2]; unsigned in[2]; \
        SB; \
        MM(0, 0); \
        if (s < 7 || !(LAST)) bn[0] = ldB(smem, a0, a1, (sn >> 2) * 256); \
        SB; \
        MM(0, 1); \
        if (s < 7 || !(LAST)) bn[1] = ldB(smem, a0, a1, (sn >> 2) * 256 + 32768); \
        SB; \
        MM(1, 0); \
        if (pf && (sp & 1) == 0) { in[0] = LDI(l4 + ((sp & 7) >> 1) * 256, pq); in[1] = LDI(l4 + ((sp & 7) >> 1) * 256, pq + 4 * PC_SIZE); } \
        if (pf) an[0] = LDW(l16 + (sp & 7) * 1024, pq); \
        SB; \
        MM(1, 1); \
        if (pf) an[1] = LDW(l16 + (sp & 7) * 1024, pq + 4 * PC_SIZE); \
        if (pf) { a[sp & 7][0] = an[0]; a[sp & 7][1] = an[1]; } \
        if (pf && (sp & 1) == 0) { ix[(sp & 7) >> 1][0] = in[0]; ix[(sp & 7) >> 1][1] = in[1]; } \
        if (s < 7 || !(LAST)) { bc[0] = bn[0]; bc[1] = bn[1]; } \
      }
#define ITER_SETUP \
        const unsigned xb = rowb + (unsigned)((kq >> 1) * 65536 + (kq & 1) * 512); \
        const unsigned xbn = rowb + (unsigned)((kqn >> 1) * 65536 + (kqn & 1) * 512); \
        unsigned xa[8]; _Pragma("unroll") for (int c = 0; c < 8; ++c) xa[c] = xb + (((unsigned)(2 * c) ^ u) << 4); \
        const unsigned xn0 = xbn + (u << 4), xn1 = xbn + ((2u ^ u) << 4);
#pragma unroll 1
      for (int it = 0; it < 3; ++it) {
        const int kq = (kq0 + it) & 3, kqn = (kq + 1) & 3;
        const int pb = pb0 + kq * PC_SIZE, pbn = pb0 + kqn * PC_SIZE;
        ITER_SETUP
        STEP(false)
      }
      {
        const int kq = (kq0 + 3) & 3, kqn = kq;
        const int pb = pb0 + kq * PC_SIZE, pbn = pb;
        ITER_SETUP
        STEP(true)
      }
      SB;
      __builtin_amdgcn_s_setprio(0);
  }
  STAMP(5);
  if (!late) tie_phase();
  {
    u16* ob = out + (size_t)(mt * 64) * Ntot + NB0 * 32;
    const __amdgpu_buffer_rsrc_t ro = __builtin_amdgcn_make_buffer_rsrc((void*)ob, 0, 64 * Ntot * 2 - NB0 * 64, 0x00020000);
#pragma unroll
    for (int mb = 0; mb < 2; ++mb) {
#pragma unroll
      for (int nb = 0; nb < 2; ++nb)
#pragma unroll
        for (int hf = 0; hf < 2; ++hf) {
          u32x4 v;
#pragma unroll
          for (int i = 0; i < 4; ++i) {
            const f32x2 f = {acc[nb][mb][hf * 8 + 2 * i], acc[nb][mb][hf * 8 + 2 * i + 1]};
            v[i] = __builtin_bit_cast(unsigned, __builtin_convertvector(f, bf16x2));
          }
          const int ch = nb * 4 + hb * 2 + hf;
          *(u32x4*)(stg + ml * 128 + ((ch ^ (ml & 7)) << 4)) = v;
        }
#pragma unroll
      for (int i = 0; i < 4; ++i) {
        const int row = (lane >> 3) + 8 * i;
        const u32x4 v = *(const u32x4*)(stg + row * 128 + (((lane & 7) ^ (row & 7)) << 4));
        __builtin_amdgcn_raw_buffer_store_b128(v, ro, ((mb * 32 + row) * Ntot + (lane & 7) * 8) * 2, 0, STAUX);
      }
    }
  }
  STAMP(6);
  STAMP(7);
  STAMP_FLUSH(8);
}

extern "C" void kernel_launch(void* const* d_in, const int* in_sizes, int n_in, void* d_out, int out_size, void* d_ws, size_t ws_size, hipStream_t stream) {
  (void)hipFuncSetAttribute((const void*)sp24_gemm, hipFuncAttributeMaxDynamicSharedMemorySize, 163840);
  sp24_gemm<<<256, 512, 163840, stream>>>((const u16*)d_in[0], (const u16*)d_in[1], (const u16*)d_in[2], (u16*)d_out, (char*)d_ws);
}
```

```cpp
#define PROBE 0
#include <hip/hip_runtime.h>
#include <hip/hip_bf16.h>
typedef unsigned short u16;
typedef __bf16 bf16x8 __attribute__((ext_vector_type(8)));
typedef float f32x4 __attribute__((ext_vector_type(4)));
typedef unsigned u32x4 __attribute__((ext_vector_type(4)));
typedef unsigned u32x2 __attribute__((ext_vector_type(2)));

constexpr int Mtot = 8192, Ntot = 1024, Ktot = 1024;

constexpr int BM = 128, BK = 64;
__device__ __forceinline__ int swz(int r, int c16) { return r * 128 + ((c16 ^ (r & 7)) << 4); }

__global__ __launch_bounds__(256, 2) void gemm_kernel(const u16* __restrict__ x, const u16* __restrict__ w, const u16* __restrict__ bias, u16* __restrict__ out) {
  extern __shared__ __attribute__((aligned(16))) char smem[];
  const unsigned long long t_start = __builtin_amdgcn_s_memrealtime();
  int bid = blockIdx.x;
  int xcd = bid & 7, off = bid >> 3; int wg = xcd * 64 + off;
  int pm = wg >> 3, pn = wg & 7;
  int brow = pm * BM, bcol = pn * BM;
  int tid = threadIdx.x, wid = tid >> 6, lane = tid & 63, wr = wid >> 1, wc = wid & 1, fr = lane & 15, fq = lane >> 4;
  auto stage = [&](int buf, int kt) {
    char* SA = smem + buf * 32768; char* SB = SA + 16384;
#pragma unroll
    for (int i = 0; i < 4; ++i) {
      int b = tid * 16 + i * 4096; int r = b >> 7, c16 = ((b >> 4) & 7) ^ (r & 7);
      __builtin_amdgcn_global_load_lds((const unsigned*)(x + (size_t)(brow + r) * Ktot + kt * BK + c16 * 8), (unsigned*)(SA + b), 16, 0, 0);
    }
    {
      int n = tid & 127;
#pragma unroll
      for (int i = 0; i < 4; ++i) {
        int c16 = (tid >> 7) * 4 + i; int k0 = kt * BK + c16 * 8;
        unsigned o[8];
#pragma unroll
        for (int g = 0; g < 2; ++g) {
          unsigned v[4], m[4];
#pragma unroll
          for (int j = 0; j < 4; ++j) { v[j] = w[(size_t)(k0 + g * 4 + j) * Ntot + bcol + n]; m[j] = v[j] & 0x7FFF; }
          unsigned hi01 = max(m[0], m[1]), lo01 = min(m[0], m[1]), hi23 = max(m[2], m[3]), lo23 = min(m[2], m[3]);
          unsigned thr = max(min(hi01, hi23), max(lo01, lo23));
#pragma unroll
          for (int j = 0; j < 4; ++j) o[g * 4 + j] = (m[j] >= thr) ? v[j] : 0u;
        }
        u32x4 r; r.x = o[0] | (o[1] << 16); r.y = o[2] | (o[3] << 16); r.z = o[4] | (o[5] << 16); r.w = o[6] | (o[7] << 16);
        *(u32x4*)(SB + swz(n, c16)) = r;
      }
    }
  };
  f32x4 acc[4][4] = {};
  stage(0, 0);
  asm volatile("s_waitcnt vmcnt(0)" ::: "memory"); __syncthreads();
  for (int t = 0; t < Ktot / BK; ++t) {
    int cur = t & 1;
    if (t + 1 < Ktot / BK) stage(cur ^ 1, t + 1);
    char* SA = smem + cur * 32768; char* SB = SA + 16384;
#pragma unroll
    for (int kk = 0; kk < 2; ++kk) {
      bf16x8 a[4], b[4];
#pragma unroll
      for (int m = 0; m < 4; ++m) a[m] = *(const bf16x8*)(SA + swz(wr * 64 + m * 16 + fr, kk * 4 + fq));
#pragma unroll
      for (int n = 0; n < 4; ++n) b[n] = *(const bf16x8*)(SB + swz(wc * 64 + n * 16 + fr, kk * 4 + fq));
#pragma unroll
      for (int m = 0; m < 4; ++m)
#pragma unroll
        for (int n = 0; n < 4; ++n)
          acc[m][n] = __builtin_amdgcn_mfma_f32_16x16x32_bf16(b[n], a[m], acc[m][n], 0, 0, 0);
    }
    asm volatile("s_waitcnt vmcnt(0)" ::: "memory"); __syncthreads();
  }
#pragma unroll
  for (int n = 0; n < 4; ++n) {
    int col = bcol + wc * 64 + n * 16 + fq * 4;
    u32x2 bb = *(const u32x2*)(bias + col);
    float b0 = __uint_as_float(bb.x << 16), b1 = __uint_as_float(bb.x & 0xFFFF0000u), b2 = __uint_as_float(bb.y << 16), b3 = __uint_as_float(bb.y & 0xFFFF0000u);
#pragma unroll
    for (int m = 0; m < 4; ++m) {
      int row = brow + wr * 64 + m * 16 + fr;
      float v0 = (float)(__bf16)acc[m][n][0] + b0, v1 = (float)(__bf16)acc[m][n][1] + b1, v2 = (float)(__bf16)acc[m][n][2] + b2, v3 = (float)(__bf16)acc[m][n][3] + b3;
      __bf16 o0 = (__bf16)v0, o1 = (__bf16)v1, o2 = (__bf16)v2, o3 = (__bf16)v3;
      u32x2 o; o.x = __builtin_bit_cast(u16, o0) | ((unsigned)__builtin_bit_cast(u16, o1) << 16); o.y = __builtin_bit_cast(u16, o2) | ((unsigned)__builtin_bit_cast(u16, o3) << 16);
      *(u32x2*)(out + (size_t)row * Ntot + col) = o;
    }
  }
}

extern "C" void kernel_launch(void* const* d_in, const int* in_sizes, int n_in, void* d_out, int out_size, void* d_ws, size_t ws_size, hipStream_t stream) {
  const u16* x = (const u16*)d_in[0]; const u16* w = (const u16*)d_in[1]; const u16* b = (const u16*)d_in[2];

  hipFuncSetAttribute((const void*)gemm_kernel, hipFuncAttributeMaxDynamicSharedMemorySize, 65536);
  gemm_kernel<<<512, 256, 65536, stream>>>(x, w, b, (u16*)d_out);
}
```

```cpp
#include <hip/hip_runtime.h>
typedef unsigned short u16;
typedef unsigned long long u64;
typedef __bf16 bf16x8 __attribute__((ext_vector_type(8)));
typedef __bf16 bf16x16 __attribute__((ext_vector_type(16)));
typedef __bf16 bf16x2 __attribute__((ext_vector_type(2)));
typedef float f32x2 __attribute__((ext_vector_type(2)));
typedef float f32x4 __attribute__((ext_vector_type(4)));
typedef unsigned u32x2 __attribute__((ext_vector_type(2)));
typedef unsigned u32x4 __attribute__((ext_vector_type(4)));
typedef unsigned u32x8 __attribute__((ext_vector_type(8)));
typedef unsigned short u16x2 __attribute__((ext_vector_type(2)));
typedef short s16x2 __attribute__((ext_vector_type(2)));
typedef __attribute__((address_space(1))) unsigned gu32;
typedef __attribute__((address_space(1))) u64 gu64;

constexpr int Ntot = 1024, Ktot = 1024;
constexpr int PC_VAL = 0, PC_IDX = 8192, PC_TR = 9216  , PC_SM = 9408, PC_S2V = 10240, PC_SIZE = 18432;
constexpr int PL_WT = 3072, PL_CT = 3088, PL_DN = 3092, PL_XR = 3096;
constexpr int WS_COPY = 128 * PC_SIZE;
constexpr int WS_FLAG = 2 * WS_COPY, WS_EPOCH = WS_FLAG + 4096;
constexpr int SC1 = 16;
#define DEPTH_ 3
#define LATE_COND (wv >= 4)
#define XP 8
#define XROT 8
constexpr int DEPTH = DEPTH_;
#define SPIN_MAX 8192

#define STAMP(i)
#define STAMP_FLUSH(n)

__device__ __forceinline__ unsigned pk_max(unsigned a, unsigned b) { return __builtin_bit_cast(unsigned, __builtin_elementwise_max(__builtin_bit_cast(u16x2, a), __builtin_bit_cast(u16x2, b))); }
__device__ __forceinline__ unsigned pk_min(unsigned a, unsigned b) { return __builtin_bit_cast(unsigned, __builtin_elementwise_min(__builtin_bit_cast(u16x2, a), __builtin_bit_cast(u16x2, b))); }
__device__ __forceinline__ unsigned pk_ltmask(unsigned a, unsigned b) {
  s16x2 d = __builtin_bit_cast(s16x2, a) - __builtin_bit_cast(s16x2, b); d = d >> (s16x2){15, 15};
  unsigned r = __builtin_bit_cast(unsigned, d); asm("" : "+v"(r)); return r; }
__device__ __forceinline__ unsigned bfi(unsigned m, unsigned x, unsigned y) { return (m & x) | (~m & y); }
__device__ __forceinline__ void prune2(unsigned a, unsigned b, unsigned c, unsigned d, unsigned& v0, unsigned& v1, unsigned& ni, unsigned& eb, unsigned& ec) {
  const unsigned ma = a & 0x7FFF7FFFu, mb = b & 0x7FFF7FFFu, mc = c & 0x7FFF7FFFu, md = d & 0x7FFF7FFFu;
  const unsigned h01 = pk_max(ma, mb), l01 = pk_min(ma, mb), h23 = pk_max(mc, md), l23 = pk_min(mc, md);
  unsigned thr = pk_max(pk_min(h01, h23), pk_max(l01, l23));
  thr = pk_max(thr, 0x00010001u);
  const unsigned na = pk_ltmask(ma, thr), nb = pk_ltmask(mb, thr), nc = pk_ltmask(mc, thr), nd = pk_ltmask(md, thr);
  v0 = bfi(na, bfi(nb, c, b), a);
  v1 = bfi(nd, bfi(nc, b, c), d) & ~(na & nd & (nb ^ nc));
  ni = (na & bfi(nb, 0x00020002u, 0x00010001u)) | bfi(nd, bfi(nc, 0x00040004u, 0x00080008u), 0x000C000Cu);
  eb = ~nb & ~na & ~(nc & nd); ec = ~nc & ~nd & ~(na & nb);
}

#define SB __builtin_amdgcn_sched_barrier(0)
#define SMF(A, B, C, I, AB) __builtin_amdgcn_smfmac_f32_16x16x64_bf16(__builtin_bit_cast(bf16x8, A), __builtin_bit_cast(bf16x16, B), C, (int)(I), 0, AB)
#define LDW(off, soff) __builtin_amdgcn_raw_buffer_load_b128(rs, off, soff, SC1)
#define LDI2(off, soff) __builtin_amdgcn_raw_buffer_load_b64(rs, off, soff, SC1)
#define LDI(off, soff) __builtin_amdgcn_raw_buffer_load_b32(rs, off, soff, SC1)
__device__ __forceinline__ unsigned lds_load(unsigned off) { unsigned v; asm volatile("ds_read_b32 %0, %1\n\ts_waitcnt lgkmcnt(0)" : "=v"(v) : "v"(off) : "memory"); return v; }
__device__ __forceinline__ unsigned lds_add(unsigned off) { unsigned v; asm volatile("ds_add_rtn_u32 %0, %1, %2\n\ts_waitcnt lgkmcnt(0)" : "=v"(v) : "v"(off), "v"(1u) : "memory"); return v; }
#define LDS_LOAD(p) lds_load(131072u + (p))
#define LDS_ADD(p) lds_add(131072u + (p))

__device__ __forceinline__ u32x8 ldB(const char* smem, unsigned a0, unsigned a1, int off) {
  const u32x4 lo = *(const u32x4*)(smem + a0 + off), hi = *(const u32x4*)(smem + a1 + off);
  return (u32x8){lo.x, lo.y, lo.z, lo.w, hi.x, hi.y, hi.z, hi.w};
}

__global__ __launch_bounds__(512, 2) void sp24_gemm(const u16* __restrict__ x, const u16* __restrict__ w, const u16* __restrict__ bias,
                                                    u16* __restrict__ out, char* ws) {
  extern __shared__ __attribute__((aligned(16))) char smem[];
  const int tid = threadIdx.x, lane = tid & 63;
  char* img = smem + 131072;
  STAMP(0);
  if (tid == 0) { *(u32x4*)(img + PL_CT) = (u32x4){0u, 0u, 0u, 0u}; }
  asm volatile("" :: "s"(x), "s"(w), "s"(bias), "s"(out), "s"(ws));
  const int bid = blockIdx.x, xcd = bid & 7, jj = bid >> 3;
  const int mt = xcd * 16 + (jj >> 1), nh = jj & 1, cp = xcd >> 2;
  const int grp = cp * 2 + nh, rank = (xcd & 3) * 16 + (jj >> 1);
  const int wv = __builtin_amdgcn_readfirstlane(tid >> 6);
  gu64* flags = (gu64*)(ws + WS_FLAG + grp * 512);
  gu32* epoch = (gu32*)(ws + WS_EPOCH + grp * 256);
  const int gp = (tid >> 4) & 15, o = tid & 15, kb = rank & 7;
  u32x2 r[8];
  if (wv < 4) {
    const u16* s0 = w + (size_t)(kb * 128 + gp * 8) * Ntot + nh * 512 + (rank >> 3) * 64 + o * 4;
    const u16* s1 = s0 + 2 * Ntot; const u16* s2 = s0 + 4 * Ntot; const u16* s3 = s0 + 6 * Ntot;
    asm volatile("global_load_dwordx2 %0, %8, off nt\n\tglobal_load_dwordx2 %1, %8, off offset:2048 nt\n\t"
                 "global_load_dwordx2 %2, %9, off nt\n\tglobal_load_dwordx2 %3, %9, off offset:2048 nt\n\t"
                 "global_load_dwordx2 %4, %10, off nt\n\tglobal_load_dwordx2 %5, %10, off offset:2048 nt\n\t"
                 "global_load_dwordx2 %6, %11, off nt\n\tglobal_load_dwordx2 %7, %11, off offset:2048 nt"
                 : "=&v"(r[0]), "=&v"(r[1]), "=&v"(r[2]), "=&v"(r[3]), "=&v"(r[4]), "=&v"(r[5]), "=&v"(r[6]), "=&v"(r[7])
                 : "v"(s0), "v"(s1), "v"(s2), "v"(s3) : "memory");
  }
  asm volatile("s_waitcnt lgkmcnt(0)" ::: "memory");
  __builtin_amdgcn_s_barrier();
  unsigned ep;
  asm volatile("s_load_dword %0, %1, 0x0" : "=s"(ep) : "s"(epoch) : "memory");
  {
    const int cnt = wv < 4 ? XP : 32 - XP, t0 = wv < 4 ? 128 - 4 * XP + wv * XP : (wv - 4) * (32 - XP);
    const u16* xb = x + (size_t)(mt * 64) * Ktot;
#pragma unroll 2
    for (int i = 0; i < cnt; i += 2) {
      const int row = ((t0 + i) >> 1) ^ (nh * XROT);
      const u16* src = xb + (size_t)row * Ktot + ((lane ^ (row & 15)) << 3);
      char* dst = smem + row * 1024;
      __builtin_amdgcn_global_load_lds((const unsigned*)src, (unsigned*)dst, 16, 0, 0);
      __builtin_amdgcn_global_load_lds((const unsigned*)(src + 512), (unsigned*)(dst + 65536), 16, 0, 0);
    }
  }
  const __amdgpu_buffer_rsrc_t rs = __builtin_amdgcn_make_buffer_rsrc((void*)(ws + (size_t)cp * WS_COPY + (size_t)nh * 64 * PC_SIZE), 0, 64 * PC_SIZE, 0x00020000);
  const unsigned l16 = lane * 16;
  char* stg = img + wv * 4096;
  const int ml = lane & 15, lq = lane >> 4;
  const int n0 = nh * 512 + wv * 64;
  u32x4 bq[2];
  if (wv < 4) {
    asm volatile("s_waitcnt vmcnt(%9) lgkmcnt(0)" : "+v"(r[0]), "+v"(r[1]), "+v"(r[2]), "+v"(r[3]), "+v"(r[4]), "+v"(r[5]), "+v"(r[6]), "+v"(r[7]), "+s"(ep) : "n"(XP) : "memory");
    const int pdst = rank * PC_SIZE;
    const int jp = gp & 1, nbp = o & 3;
    const int fl0 = 4 * (o >> 2) + 16 * ((gp >> 1) & 1);
    unsigned emask = 0;
    unsigned V0[2][2], V1[2][2], NI[2][2];
#pragma unroll
    for (int g = 0; g < 2; ++g)
#pragma unroll
      for (int pp = 0; pp < 2; ++pp) {
        unsigned eb, ec;
        prune2(r[g * 4 + 0][pp], r[g * 4 + 1][pp], r[g * 4 + 2][pp], r[g * 4 + 3][pp], V0[g][pp], V1[g][pp], NI[g][pp], eb, ec);
        const unsigned e2 = (eb & 0x00020001u) | (ec & 0x00080004u);
        emask |= ((e2 | (e2 >> 16)) & 15u) << (g * 8 + pp * 4);
      }
    char* fp = stg + nbp * 512 + fl0 * 16 + jp * 8;
#pragma unroll
    for (int pp = 0; pp < 2; ++pp) {
      u32x2 lo, hi;
      lo.x = __builtin_amdgcn_perm(V1[0][pp], V0[0][pp], 0x05040100u); lo.y = __builtin_amdgcn_perm(V1[1][pp], V0[1][pp], 0x05040100u);
      hi.x = __builtin_amdgcn_perm(V1[0][pp], V0[0][pp], 0x07060302u); hi.y = __builtin_amdgcn_perm(V1[1][pp], V0[1][pp], 0x07060302u);
      *(u32x2*)(fp + pp * 32) = lo; *(u32x2*)(fp + pp * 32 + 16) = hi;
      const unsigned by = NI[0][pp] | (NI[1][pp] << 4);
      char* q = stg + 2048 + (fl0 + 2 * pp) * 8 + nbp * 2 + jp;
      *(unsigned char*)q = (unsigned char)by; *(unsigned char*)(q + 8) = (unsigned char)(by >> 16);
    }
    int tot = 0;
    for (unsigned rem = emask;;) { const u64 bl = __ballot(rem != 0); if (!bl) break; tot += __popcll(bl); rem &= rem - 1; }
    if (lane == 0) { *(int*)(img + PL_WT + wv * 4) = tot; asm volatile("s_waitcnt lgkmcnt(0)" ::: "memory"); LDS_ADD(PL_CT); }
    const unsigned voff = (unsigned)((wv >> 1) * 4096 + (wv & 1) * 512 + (lane >> 5) * 1024 + (lane & 31) * 16);
    __builtin_amdgcn_raw_buffer_store_b128(*(const u32x4*)(stg + l16), rs, PC_VAL + voff, pdst, SC1);
    __builtin_amdgcn_raw_buffer_store_b128(*(const u32x4*)(stg + 1024 + l16), rs, PC_VAL + 2048 + voff, pdst, SC1);
    __builtin_amdgcn_raw_buffer_store_b32(*(const unsigned*)(stg + 2048 + lane * 4), rs, PC_IDX + wv * 256 + lane * 4, pdst, SC1);
    STAMP(1);
    while (LDS_LOAD(PL_CT) < 4u) {}
    asm volatile("" ::: "memory");
    const u32x4 wt = *(const u32x4*)(img + PL_WT);
    const int total = (int)(wt.x + wt.y + wt.z + wt.w);
    const unsigned rtag = (ep + 1u) * 0x9E3779B1u ^ 0x7F4A7C15u;
    if (wv == 0 && lane >= total && lane < 24) __builtin_amdgcn_raw_buffer_store_b64((u32x2){0u, rtag}, rs, PC_TR + lane * 8, pdst, SC1);
    unsigned s2mask = 0;
    int base = (wv > 0 ? (int)wt.x : 0) + (wv > 1 ? (int)wt.y : 0) + (wv > 2 ? (int)wt.z : 0);
    for (unsigned rem = emask;;) {
      const u64 bl = __ballot(rem != 0); if (!bl) break;
      if (rem != 0) {
        const int q = base + __builtin_amdgcn_mbcnt_hi((unsigned)(bl >> 32), __builtin_amdgcn_mbcnt_lo((unsigned)bl, 0));
        const int bit = __builtin_ctz(rem);
        if (q < 24) {
          const int g = bit >> 3, pp = (bit >> 2) & 1, e = (bit >> 1) & 1, hf = bit & 1;
          unsigned dv = 0;
#pragma unroll
          for (int gg = 0; gg < 2; ++gg)
#pragma unroll
            for (int ee = 0; ee < 2; ++ee)
#pragma unroll
              for (int p2 = 0; p2 < 2; ++p2) dv = (g == gg && e == ee && pp == p2) ? r[gg * 4 + 1 + ee][p2] : dv;
          const unsigned val = hf ? (dv >> 16) : (dv & 0xFFFFu);
          const unsigned gi = (unsigned)(kb * 32 + gp * 2 + g);
          const unsigned rowid = (unsigned)(nbp * 16 + 4 * (o >> 2) + 2 * pp + hf);
          __builtin_amdgcn_raw_buffer_store_b64((u32x2){(val << 16) | ((unsigned)e << 15) | (rowid << 8) | gi, rtag}, rs, PC_TR + q * 8, pdst, SC1);
        } else s2mask |= 1u << bit;
      }
      base += __popcll(bl); rem &= rem - 1;
    }
    unsigned nbm = 0;
    if (total > 24) {
      const u64 bl = __ballot(s2mask != 0);
#pragma unroll
      for (int i = 0; i < 4; ++i) nbm |= (bl & (0x1111111111111111ull << i)) ? (1u << i) : 0u;
#pragma unroll
      for (int pp = 0; pp < 2; ++pp) {
        unsigned wv2[2][2];
#pragma unroll
        for (int g = 0; g < 2; ++g) {
          const unsigned bb = r[g * 4 + 1][pp], cc = r[g * 4 + 2][pp];
          const unsigned bits = (s2mask >> (g * 8 + pp * 4)) & 15u;
          wv2[0][g] = ((bits & 1u) ? (bb & 0xFFFFu) : 0u) | ((bits & 4u) ? (cc << 16) : 0u);
          wv2[1][g] = ((bits & 2u) ? (bb >> 16) : 0u) | ((bits & 8u) ? (cc & 0xFFFF0000u) : 0u);
        }
        *(u32x2*)(fp + pp * 32) = (u32x2){wv2[0][0], wv2[0][1]}; *(u32x2*)(fp + pp * 32 + 16) = (u32x2){wv2[1][0], wv2[1][1]};
      }
      __builtin_amdgcn_raw_buffer_store_b128(*(const u32x4*)(stg + l16), rs, PC_S2V + voff, pdst, SC1);
      __builtin_amdgcn_raw_buffer_store_b128(*(const u32x4*)(stg + 1024 + l16), rs, PC_S2V + 2048 + voff, pdst, SC1);
    }
    if (lane == 0) __builtin_amdgcn_raw_buffer_store_b8((unsigned char)nbm, rs, PC_SM + wv, pdst, SC1);
    asm volatile("s_waitcnt vmcnt(0)" ::: "memory");
    STAMP(2);
    if (lane == 0) {
      LDS_ADD(PL_XR);
      const unsigned t0 = ep + 1u;
      if (LDS_ADD(PL_DN) == 3u) __hip_atomic_store(flags + rank, ((u64)(t0 * 0x9E3779B1u ^ 0x7F4A7C15u) << 32) | t0, __ATOMIC_RELAXED, __HIP_MEMORY_SCOPE_AGENT);
    }
    { const u32x4* bp = (const u32x4*)(bias + n0 + lq * 16); bq[0] = bp[0]; bq[1] = bp[1]; }
  } else {
    STAMP(1);
    asm volatile("s_waitcnt vmcnt(0) lgkmcnt(0)" : "+s"(ep) :: "memory");
    if (lane == 0) LDS_ADD(PL_XR);
    STAMP(2);
    { const u32x4* bp = (const u32x4*)(bias + n0 + lq * 16); bq[0] = bp[0]; bq[1] = bp[1]; }
  }
  const int pb0 = wv * 8 * PC_SIZE;
  f32x4 acc[4][4];
  auto init_acc = [&]() __attribute__((always_inline)) {
    const unsigned bw[8] = {bq[0].x, bq[0].y, bq[0].z, bq[0].w, bq[1].x, bq[1].y, bq[1].z, bq[1].w};
#pragma unroll
    for (int nb = 0; nb < 4; ++nb) {
      const f32x4 bv = {__uint_as_float(bw[2 * nb] << 16), __uint_as_float(bw[2 * nb] & 0xFFFF0000u), __uint_as_float(bw[2 * nb + 1] << 16), __uint_as_float(bw[2 * nb + 1] & 0xFFFF0000u)};
#pragma unroll
      for (int mb = 0; mb < 4; ++mb) acc[nb][mb] = bv;
    }
  };
  init_acc();
  const unsigned rowb = ml * 1024;
  const unsigned u = (unsigned)(lq ^ ml);
#define WAIT_X() do { while (LDS_LOAD(PL_XR) < 8u) __builtin_amdgcn_s_sleep(1); asm volatile("" ::: "memory"); } while (0)

  const unsigned tg0 = ep + 1u, tg1 = tg0 * 0x9E3779B1u ^ 0x7F4A7C15u;
  auto tie_phase = [&]() __attribute__((always_inline)) -> bool {
  bool okr = false;
  unsigned rec[3];
#pragma unroll 1
  for (int spin = 0; spin < SPIN_MAX; ++spin) {
    const u32x2 g0 = LDI2(lq * PC_SIZE + PC_TR + ml * 8, pb0), g1 = LDI2(lq * PC_SIZE + PC_TR + ml * 8, pb0 + 4 * PC_SIZE);
    const u32x2 g2 = LDI2((lane >> 3) * PC_SIZE + PC_TR + 128 + (lane & 7) * 8, pb0);
    rec[0] = g0.x; rec[1] = g1.x; rec[2] = g2.x;
    if (__ballot(g0.y == tg1 && g1.y == tg1 && g2.y == tg1) == ~0ull) { okr = true; break; }
    asm volatile("" ::: "memory");
    __builtin_amdgcn_s_sleep(1);
  }
  if (okr) {
    *(u32x4*)(stg + l16) = (u32x4){0, 0, 0, 0};
    *(u32x4*)(stg + 1024 + l16) = (u32x4){0, 0, 0, 0};
    const unsigned jr = (unsigned)((lq & 1) * 4 + (ml >> 2)), sq = (unsigned)(ml & 3);
    const unsigned qa = (jr >> 2) * 2u + (sq >> 1), el = 4u * (sq & 1u) + (jr & 3u);
    unsigned ap[3]; u64 cls[3];
    const u64 xtra = __ballot(rec[2] != 0u);
#pragma unroll
    for (int i = 0; i < 3; ++i) {
      const unsigned gi = rec[i] & 255u;
      *(unsigned*)(stg + 2048 + (((i * 2 + (lq >> 1)) * 4 + sq) * 8 + jr) * 4) = ((gi & 128u) << 9) | ((gi & 127u) << 3);
      ap[i] = ((rec[i] >> 12) & 1u) * 1024u + (((rec[i] >> 8) & 15u) + 16u * qa) * 16u + el * 2u;
      cls[i] = __ballot((rec[i] >> 15) & 1u);
    }
    const unsigned tb = rowb + (unsigned)(ml << 4);
    WAIT_X();
#pragma unroll
    for (int bt = 0; bt < 6; ++bt) {
      if (bt >= 4 && (unsigned)(xtra >> (32 * (bt & 1))) == 0u) continue;
      const int ri = bt >> 1;
      const bool mine = (lq >> 1) == (bt & 1);
      const u32x4 k0 = *(const u32x4*)(stg + 2048 + bt * 128 + lq * 32), k1 = *(const u32x4*)(stg + 2048 + bt * 128 + lq * 32 + 16);
      const unsigned ko[8] = {k0.x, k0.y, k0.z, k0.w, k1.x, k1.y, k1.z, k1.w};
      u32x2 g[4][8];
#pragma unroll
      for (int j = 0; j < 8; ++j) {
        const unsigned ad = tb ^ ko[j];
        asm volatile("ds_read_b64 %0, %4\n\tds_read_b64 %1, %4 offset:16384\n\tds_read_b64 %2, %4 offset:32768\n\tds_read_b64 %3, %4 offset:49152"
                     : "=&v"(g[0][j]), "=&v"(g[1][j]), "=&v"(g[2][j]), "=&v"(g[3][j]) : "v"(ad) : "memory");
      }
      const unsigned c32 = (unsigned)(cls[ri] >> (32 * (bt & 1))) >> (16 * (lq >> 1) + 2 * (lq & 1));
      const unsigned lo4 = c32 & 0x1111u, hi4 = (c32 >> 1) & 0x1111u;
      const unsigned l2 = (lo4 | (lo4 >> 2)) & 0x0505u, h2 = (hi4 | (hi4 >> 2)) & 0x0505u;
      const unsigned ti = 0x8888u | ((l2 | (l2 >> 4)) & 0x55u) | (((h2 | (h2 >> 4)) & 0x55u) << 8);
      u32x4 at[4];
#pragma unroll
      for (int hf = 0; hf < 2; ++hf) {
        const unsigned aw = (mine && ((rec[ri] >> 13) & 1u) == (unsigned)hf) ? ap[ri] : 2816u + (unsigned)lane * 2u;
        *(u16*)(stg + aw) = (u16)(rec[ri] >> 16);
        asm volatile("" ::: "memory");
        at[2 * hf] = *(const u32x4*)(stg + l16); at[2 * hf + 1] = *(const u32x4*)(stg + 1024 + l16);
        asm volatile("" ::: "memory");
        *(u16*)(stg + aw) = (u16)0;
      }
      asm volatile("s_waitcnt lgkmcnt(0)"
                   : "+v"(g[0][0]), "+v"(g[0][1]), "+v"(g[0][2]), "+v"(g[0][3]), "+v"(g[0][4]), "+v"(g[0][5]), "+v"(g[0][6]), "+v"(g[0][7]),
                     "+v"(g[1][0]), "+v"(g[1][1]), "+v"(g[1][2]), "+v"(g[1][3]), "+v"(g[1][4]), "+v"(g[1][5]), "+v"(g[1][6]), "+v"(g[1][7]) :: "memory");
      asm volatile("" : "+v"(g[2][0]), "+v"(g[2][1]), "+v"(g[2][2]), "+v"(g[2][3]), "+v"(g[2][4]), "+v"(g[2][5]), "+v"(g[2][6]), "+v"(g[2][7]),
                        "+v"(g[3][0]), "+v"(g[3][1]), "+v"(g[3][2]), "+v"(g[3][3]), "+v"(g[3][4]), "+v"(g[3][5]), "+v"(g[3][6]), "+v"(g[3][7]) :: "memory");
#pragma unroll
      for (int mb = 0; mb < 4; ++mb) {
        u32x8 b8;
#pragma unroll
        for (int j = 0; j < 8; ++j) b8[j] = __builtin_amdgcn_alignbit(g[mb][j].y, g[mb][j].x, 16);
#pragma unroll
        for (int nb = 0; nb < 4; ++nb) acc[nb][mb] = SMF(at[nb], b8, acc[nb][mb], ti, 0);
      }
    }
  }
  return okr;
  };
  const bool late = LATE_COND;
  bool ok = true;
  STAMP(3);
  if (late) ok = tie_phase();
  STAMP(4);
  if (ok) {
    ok = false;
    const u64 tag = ((u64)tg1 << 32) | tg0;
#pragma unroll 1
    for (int spin = 0; spin < SPIN_MAX; ++spin) {
      const u64 f = __hip_atomic_load(flags + wv * 8 + (lane & 7), __ATOMIC_RELAXED, __HIP_MEMORY_SCOPE_AGENT);
      if (__ballot(f == tag) == ~0ull) { ok = true; break; }
      __builtin_amdgcn_s_sleep(1);
    }
    __builtin_amdgcn_fence(__ATOMIC_ACQUIRE, "wavefront");
  }
  STAMP(5);
  if (ok) {
    const unsigned smk = LDI((lane & 7) * PC_SIZE + PC_SM, pb0);
    {
      const int q0 = (mt >> 1) & 3;
      const unsigned l8 = lane * 8 + PC_IDX;
      u32x4 a[4][4]; u32x2 ix[4];
      {
        const int pb = pb0 + q0 * 2 * PC_SIZE;
#pragma unroll
        for (int s = 0; s < DEPTH; ++s) {
          ix[s] = LDI2(l8 + (s & 1) * 512, pb + (s >> 1) * PC_SIZE);
#pragma unroll
          for (int nb = 0; nb < 4; ++nb) a[s][nb] = LDW(l16 + (s & 1) * 4096 + nb * 1024, pb + (s >> 1) * PC_SIZE);
        }
      }
      WAIT_X();
      u32x8 bc[2];
      {
        const unsigned xb = rowb + (unsigned)((q0 >> 1) * 65536 + (q0 & 1) * 512);
        const unsigned a0 = xb + (u << 4), a1 = xb + ((4u ^ u) << 4);
        bc[0] = ldB(smem, a0, a1, 0); bc[1] = ldB(smem, a0, a1, 16384);
      }
#define MM(nb, mi) do { if ((nb) & 1) acc[nb][2 * hs + mi] = SMF(a[s][nb], bc[mi], acc[nb][2 * hs + mi], ix[s][(nb) >> 1], 1); \
                        else acc[nb][2 * hs + mi] = SMF(a[s][nb], bc[mi], acc[nb][2 * hs + mi], ix[s][(nb) >> 1], 0); } while (0)
#define STEP(LAST) \
      _Pragma("unroll") for (int s = 0; s < 4; ++s) { \
        const int sp = s + DEPTH, pq = (sp < 4 ? pb : pbn) + ((sp & 3) >> 1) * PC_SIZE; \
        const bool pf = sp < 4 || !(LAST); \
        u32x4 an[4]; u32x2 in; \
        _Pragma("unroll") for (int hs = 0; hs < 2; ++hs) { \
            \
          const int sn = hs ? (s + 1) & 3 : s; \
          const bool nx = hs == 0 || s < 3 || !(LAST); \
          const unsigned a0 = (hs && s == 3) ? xn0 : xa[2 * (sn & 1)], a1 = (hs && s == 3) ? xn1 : xa[2 * (sn & 1) + 1]; \
          const int xo = (sn >> 1) * 256 + (hs ? 0 : 32768); \
          u32x8 bn[2]; \
          SB; \
          MM(0, 0); MM(1, 0); \
          if (nx) bn[0] = ldB(smem, a0, a1, xo); \
          SB; \
          MM(2, 0); MM(3, 0); \
          if (nx) bn[1] = ldB(smem, a0, a1, xo + 16384); \
          SB; \
          MM(0, 1); MM(1, 1); \
          if (pf && hs == 0) in = LDI2(l8 + (sp & 1) * 512, pq); \
          if (pf) an[2 * hs] = LDW(l16 + (sp & 1) * 4096 + (2 * hs) * 1024, pq); \
          SB; \
          MM(2, 1); MM(3, 1); \
          if (pf) an[2 * hs + 1] = LDW(l16 + (sp & 1) * 4096 + (2 * hs + 1) * 1024, pq); \
          if (nx) { bc[0] = bn[0]; bc[1] = bn[1]; } \
        } \
        if (pf) { _Pragma("unroll") for (int nb = 0; nb < 4; ++nb) a[sp & 3][nb] = an[nb]; ix[sp & 3] = in; } \
      }
#define ITER_SETUP \
        const unsigned xb = rowb + (unsigned)((kq >> 1) * 65536 + (kq & 1) * 512); \
        const unsigned xbn = rowb + (unsigned)((kqn >> 1) * 65536 + (kqn & 1) * 512); \
        unsigned xa[4]; _Pragma("unroll") for (int c = 0; c < 4; ++c) xa[c] = xb + (((unsigned)(4 * c) ^ u) << 4); \
        const unsigned xn0 = xbn + (u << 4), xn1 = xbn + ((4u ^ u) << 4);
#pragma unroll 1
      for (int it = 0; it < 3; ++it) {
        const int kq = (q0 + it) & 3, kqn = (kq + 1) & 3;
        const int pb = pb0 + kq * 2 * PC_SIZE, pbn = pb0 + kqn * 2 * PC_SIZE;
        ITER_SETUP
        STEP(false)
      }
      {
        const int kq = (q0 + 3) & 3, kqn = kq;
        const int pb = pb0 + kq * 2 * PC_SIZE, pbn = pb;
        ITER_SETUP
        STEP(true)
      }
      SB;
      __builtin_amdgcn_s_setprio(0);
    }
    if (__ballot(smk != 0u)) {
#pragma unroll 1
      for (int S = 0; S < 16; ++S) {
        const unsigned mw = (unsigned)__builtin_amdgcn_readlane((int)smk, S >> 1) >> (16 * (S & 1));
        const unsigned m4 = (mw | (mw >> 8)) & 15u;
        if (m4) {
          const unsigned xb = rowb + (unsigned)((S >> 3) * 65536 + ((S & 7) >> 1) * 256);
          const unsigned c0 = (unsigned)(8 * (S & 1));
          const unsigned a0 = xb + ((c0 ^ u) << 4), a1 = xb + (((c0 + 4) ^ u) << 4);
#pragma unroll
          for (int nb = 0; nb < 4; ++nb) {
            if ((m4 >> nb) & 1u) {
              const u32x4 a2 = LDW(l16 + PC_S2V + nb * 1024, pb0 + (S >> 1) * PC_SIZE + (S & 1) * 4096);
#pragma unroll
              for (int mb = 0; mb < 4; ++mb) acc[nb][mb] = SMF(a2, ldB(smem, a0, a1, mb * 16384), acc[nb][mb], 0x99999999u, 0);
            }
          }
        }
      }
    }
    STAMP(6);
    if (!late) ok = tie_phase();
  }
  if (!ok) {
    WAIT_X();
    init_acc();
#pragma unroll 1
    for (int S = 0; S < 16; ++S) {
      const unsigned xb = rowb + (unsigned)((S >> 3) * 65536 + ((S & 7) >> 1) * 256);
      const unsigned c0 = (unsigned)(8 * (S & 1));
      const unsigned a0 = xb + ((c0 ^ u) << 4), a1 = xb + (((c0 + 4) ^ u) << 4);
#pragma unroll
      for (int nb = 0; nb < 4; ++nb) {
        const u16* src = w + (size_t)(S * 64 + lq * 16) * Ntot + n0 + 16 * (ml >> 2) + 4 * nb + (ml & 3);
        u32x4 am, a2; unsigned iw = 0;
#pragma unroll
        for (int gg = 0; gg < 2; ++gg) {
          unsigned e[4];
#pragma unroll
          for (int i = 0; i < 4; ++i) e[i] = (unsigned)src[(size_t)(gg * 8 + i) * Ntot] | ((unsigned)src[(size_t)(gg * 8 + 4 + i) * Ntot] << 16);
          unsigned v0, v1, ni, eb, ec;
          prune2(e[0], e[1], e[2], e[3], v0, v1, ni, eb, ec);
          am[2 * gg] = __builtin_amdgcn_perm(v1, v0, 0x05040100u); am[2 * gg + 1] = __builtin_amdgcn_perm(v1, v0, 0x07060302u);
          const unsigned xb2 = e[1] & eb, xc2 = e[2] & ec;
          a2[2 * gg] = __builtin_amdgcn_perm(xc2, xb2, 0x05040100u); a2[2 * gg + 1] = __builtin_amdgcn_perm(xc2, xb2, 0x07060302u);
          iw |= ((ni & 15u) | ((ni >> 12) & 0xF0u)) << (8 * gg);
        }
#pragma unroll
        for (int mb = 0; mb < 4; ++mb) {
          const u32x8 b = ldB(smem, a0, a1, mb * 16384);
          acc[nb][mb] = SMF(am, b, acc[nb][mb], iw, 0);
          acc[nb][mb] = SMF(a2, b, acc[nb][mb], 0x99999999u, 0);
        }
      }
    }
  }
  {
    u16* ob = out + (size_t)(mt * 64) * Ntot + n0;
#pragma unroll
    for (int mb = 0; mb < 4; ++mb) {
#pragma unroll
      for (int hf = 0; hf < 2; ++hf) {
        u32x4 v;
#pragma unroll
        for (int i = 0; i < 4; ++i) {
          const f32x4 c = acc[2 * hf + (i >> 1)][mb];
          const f32x2 f = {c[2 * (i & 1)], c[2 * (i & 1) + 1]};
          v[i] = __builtin_bit_cast(unsigned, __builtin_convertvector(f, bf16x2));
        }
        const int ch = lq * 2 + hf;
        *(u32x4*)(stg + ml * 128 + ((ch ^ (ml & 7)) << 4)) = v;
      }
#pragma unroll
      for (int i = 0; i < 2; ++i) {
        const int row = (lane >> 3) + 8 * i;
        const u32x4 v = *(const u32x4*)(stg + row * 128 + (((lane & 7) ^ (row & 7)) << 4));
        __builtin_nontemporal_store(v, (u32x4*)(ob + (size_t)(mb * 16 + row) * Ntot + (lane & 7) * 8));
      }
    }
  }
  if (tid == 0) __hip_atomic_fetch_add(epoch, 1u, __ATOMIC_RELAXED, __HIP_MEMORY_SCOPE_AGENT);
  STAMP(7);
  STAMP_FLUSH(8);
}

extern "C" void kernel_launch(void* const* d_in, const int* in_sizes, int n_in, void* d_out, int out_size, void* d_ws, size_t ws_size, hipStream_t stream) {
  (void)hipFuncSetAttribute((const void*)sp24_gemm, hipFuncAttributeMaxDynamicSharedMemorySize, 163840);
  sp24_gemm<<<256, 512, 163840, stream>>>((const u16*)d_in[0], (const u16*)d_in[1], (const u16*)d_in[2], (u16*)d_out, (char*)d_ws);
}
```

```cpp
#include <hip/hip_runtime.h>
typedef unsigned short u16;
typedef unsigned long long u64;
typedef __bf16 bf16x8 __attribute__((ext_vector_type(8)));
typedef __bf16 bf16x16 __attribute__((ext_vector_type(16)));
typedef __bf16 bf16x2 __attribute__((ext_vector_type(2)));
typedef float f32x2 __attribute__((ext_vector_type(2)));
typedef float f32x4 __attribute__((ext_vector_type(4)));
typedef unsigned u32x2 __attribute__((ext_vector_type(2)));
typedef unsigned u32x4 __attribute__((ext_vector_type(4)));
typedef unsigned u32x8 __attribute__((ext_vector_type(8)));
typedef unsigned short u16x2 __attribute__((ext_vector_type(2)));
typedef short s16x2 __attribute__((ext_vector_type(2)));
typedef __attribute__((address_space(1))) unsigned gu32;
typedef __attribute__((address_space(1))) u64 gu64;

constexpr int Ntot = 1024, Ktot = 1024;
constexpr int PC_VAL = 0, PC_IDX = 8192, PC_TR = 9216  , PC_SM = 9408, PC_S2V = 10240, PC_SIZE = 18432;
constexpr int PL_WT = 3072, PL_CT = 3088, PL_DN = 3092, PL_XR = 3096;
constexpr int WS_COPY = 128 * PC_SIZE;
constexpr int WS_FLAG = 2 * WS_COPY, WS_EPOCH = WS_FLAG + 65536;
constexpr int SC1 = 16;
constexpr int DEPTH = 3;
constexpr int FSTR = 16;
constexpr int XP = 8;
constexpr int SPIN_MAX = 8192;

__device__ __forceinline__ unsigned pk_max(unsigned a, unsigned b) { return __builtin_bit_cast(unsigned, __builtin_elementwise_max(__builtin_bit_cast(u16x2, a), __builtin_bit_cast(u16x2, b))); }
__device__ __forceinline__ unsigned pk_min(unsigned a, unsigned b) { return __builtin_bit_cast(unsigned, __builtin_elementwise_min(__builtin_bit_cast(u16x2, a), __builtin_bit_cast(u16x2, b))); }
__device__ __forceinline__ unsigned pk_ltmask(unsigned a, unsigned b) {
  s16x2 d = __builtin_bit_cast(s16x2, a) - __builtin_bit_cast(s16x2, b); d = d >> (s16x2){15, 15};
  unsigned r = __builtin_bit_cast(unsigned, d); asm("" : "+v"(r)); return r; }
__device__ __forceinline__ unsigned bfi(unsigned m, unsigned x, unsigned y) { return (m & x) | (~m & y); }
__device__ __forceinline__ void prune2(unsigned a, unsigned b, unsigned c, unsigned d, unsigned& v0, unsigned& v1, unsigned& ni, unsigned& eb, unsigned& ec) {
  const unsigned ma = a & 0x7FFF7FFFu, mb = b & 0x7FFF7FFFu, mc = c & 0x7FFF7FFFu, md = d & 0x7FFF7FFFu;
  const unsigned h01 = pk_max(ma, mb), l01 = pk_min(ma, mb), h23 = pk_max(mc, md), l23 = pk_min(mc, md);
  unsigned thr = pk_max(pk_min(h01, h23), pk_max(l01, l23));
  thr = pk_max(thr, 0x00010001u);
  const unsigned na = pk_ltmask(ma, thr), nb = pk_ltmask(mb, thr), nc = pk_ltmask(mc, thr), nd = pk_ltmask(md, thr);
  v0 = bfi(na, bfi(nb, c, b), a);
  v1 = bfi(nd, bfi(nc, b, c), d) & ~(na & nd & (nb ^ nc));
  ni = (na & bfi(nb, 0x00020002u, 0x00010001u)) | bfi(nd, bfi(nc, 0x00040004u, 0x00080008u), 0x000C000Cu);
  eb = ~nb & ~na & ~(nc & nd); ec = ~nc & ~nd & ~(na & nb);
}

#define SB __builtin_amdgcn_sched_barrier(0)
#define SMF(A, B, C, I, AB) __builtin_amdgcn_smfmac_f32_16x16x64_bf16(__builtin_bit_cast(bf16x8, A), __builtin_bit_cast(bf16x16, B), C, (int)(I), 0, AB)
#define LDW(off, soff) __builtin_amdgcn_raw_buffer_load_b128(rs, off, soff, SC1)
#define LDI2(off, soff) __builtin_amdgcn_raw_buffer_load_b64(rs, off, soff, SC1)
#define LDI(off, soff) __builtin_amdgcn_raw_buffer_load_b32(rs, off, soff, SC1)
__device__ __forceinline__ unsigned lds_load(unsigned off) { unsigned v; asm volatile("ds_read_b32 %0, %1\n\ts_waitcnt lgkmcnt(0)" : "=v"(v) : "v"(off) : "memory"); return v; }
__device__ __forceinline__ unsigned lds_add(unsigned off) { unsigned v; asm volatile("ds_add_rtn_u32 %0, %1, %2\n\ts_waitcnt lgkmcnt(0)" : "=v"(v) : "v"(off), "v"(1u) : "memory"); return v; }
#define LDS_LOAD(p) lds_load(131072u + (p))
#define LDS_ADD(p) lds_add(131072u + (p))

__device__ __forceinline__ u32x8 ldB(const char* smem, unsigned a0, unsigned a1, int off) {
  const u32x4 lo = *(const u32x4*)(smem + a0 + off), hi = *(const u32x4*)(smem + a1 + off);
  return (u32x8){lo.x, lo.y, lo.z, lo.w, hi.x, hi.y, hi.z, hi.w};
}

__global__ __launch_bounds__(512, 2) void sp24_gemm(const u16* __restrict__ x, const u16* __restrict__ w, const u16* __restrict__ bias,
                                                    u16* __restrict__ out, char* ws) {
  extern __shared__ __attribute__((aligned(16))) char smem[];
  const int tid = threadIdx.x, lane = tid & 63;
  char* img = smem + 131072;
  if (tid == 0) { *(u32x4*)(img + PL_CT) = (u32x4){0u, 0u, 0u, 0u}; }
  asm volatile("" :: "s"(x), "s"(w), "s"(bias), "s"(out), "s"(ws));
  const int bid = blockIdx.x, xcd = bid & 7, jj = bid >> 3;
  const int mt = xcd * 16 + (jj >> 1), nh = jj & 1, cp = xcd >> 2;
  const int grp = cp * 2 + nh, rank = (xcd & 3) * 16 + (jj >> 1);
  const int wv = __builtin_amdgcn_readfirstlane(tid >> 6);
  gu64* flags = (gu64*)(ws + WS_FLAG + grp * 512 * FSTR);
  gu32* epoch = (gu32*)(ws + WS_EPOCH + grp * 256);
  const int gp = (tid >> 4) & 15, o = tid & 15, kb = rank & 7;
  const int ml = lane & 15, lq = lane >> 4;
  const int n0 = nh * 512 + wv * 64;
  u32x2 r[8]; u32x4 bq[2]; unsigned ep;
  {
    const u16* s0 = w + (size_t)(kb * 128 + gp * 8) * Ntot + nh * 512 + (rank >> 3) * 64 + o * 4;
    const u16* s1 = s0 + 2 * Ntot; const u16* s2 = s0 + 4 * Ntot; const u16* s3 = s0 + 6 * Ntot;
    const u16* bp = bias + n0 + lq * 16;
    asm volatile("s_cmp_gt_u32 %17, 3\n\ts_cbranch_scc1 .Lskipw%=\n\t"
                 "global_load_dwordx2 %0, %11, off nt\n\tglobal_load_dwordx2 %1, %11, off offset:2048 nt\n\t"
                 "global_load_dwordx2 %2, %12, off nt\n\tglobal_load_dwordx2 %3, %12, off offset:2048 nt\n\t"
                 "global_load_dwordx2 %4, %13, off nt\n\tglobal_load_dwordx2 %5, %13, off offset:2048 nt\n\t"
                 "global_load_dwordx2 %6, %14, off nt\n\tglobal_load_dwordx2 %7, %14, off offset:2048 nt\n"
                 ".Lskipw%=:\n\t"
                 "global_load_dwordx4 %8, %15, off\n\tglobal_load_dwordx4 %9, %15, off offset:16\n\tglobal_load_dword %10, %16, off sc1"
                 : "=&v"(r[0]), "=&v"(r[1]), "=&v"(r[2]), "=&v"(r[3]), "=&v"(r[4]), "=&v"(r[5]), "=&v"(r[6]), "=&v"(r[7]), "=&v"(bq[0]), "=&v"(bq[1]), "=&v"(ep)
                 : "v"(s0), "v"(s1), "v"(s2), "v"(s3), "v"(bp), "v"(epoch), "s"(wv) : "memory", "scc");
  }
  asm volatile("s_waitcnt lgkmcnt(0)" ::: "memory");
  __builtin_amdgcn_s_barrier();
  {
    const int cnt = wv < 4 ? XP : 32 - XP, t0 = wv < 4 ? 128 - 4 * XP + wv * XP : (wv - 4) * (32 - XP);
    const u16* xb = x + (size_t)(mt * 64) * Ktot;
#pragma unroll 2
    for (int i = 0; i < cnt; i += 2) {
      const int row = (t0 + i) >> 1;
      const u16* src = xb + (size_t)row * Ktot + ((lane ^ (row & 15)) << 3);
      char* dst = smem + row * 1024;
      __builtin_amdgcn_global_load_lds((const unsigned*)src, (unsigned*)dst, 16, 0, 2);
      __builtin_amdgcn_global_load_lds((const unsigned*)(src + 512), (unsigned*)(dst + 65536), 16, 0, 2);
    }
  }
  const __amdgpu_buffer_rsrc_t rs = __builtin_amdgcn_make_buffer_rsrc((void*)(ws + (size_t)cp * WS_COPY + (size_t)nh * 64 * PC_SIZE), 0, 64 * PC_SIZE, 0x00020000);
  const unsigned l16 = lane * 16;
  char* stg = img + wv * 4096;
  if (wv < 4) {
    asm volatile("s_waitcnt vmcnt(%8)" : "+v"(r[0]), "+v"(r[1]), "+v"(r[2]), "+v"(r[3]), "+v"(r[4]), "+v"(r[5]), "+v"(r[6]), "+v"(r[7]) : "n"(XP + 3) : "memory");
    const int pdst = rank * PC_SIZE;
    const int jp = gp & 1, nbp = o & 3;
    const int fl0 = 4 * (o >> 2) + 16 * ((gp >> 1) & 1);
    unsigned emask = 0;
    unsigned V0[2][2], V1[2][2], NI[2][2];
#pragma unroll
    for (int g = 0; g < 2; ++g)
#pragma unroll
      for (int pp = 0; pp < 2; ++pp) {
        unsigned eb, ec;
        prune2(r[g * 4 + 0][pp], r[g * 4 + 1][pp], r[g * 4 + 2][pp], r[g * 4 + 3][pp], V0[g][pp], V1[g][pp], NI[g][pp], eb, ec);
        const unsigned e2 = (eb & 0x00020001u) | (ec & 0x00080004u);
        emask |= ((e2 | (e2 >> 16)) & 15u) << (g * 8 + pp * 4);
      }
    char* fp = stg + nbp * 512 + fl0 * 16 + jp * 8;
#pragma unroll
    for (int pp = 0; pp < 2; ++pp) {
      u32x2 lo, hi;
      lo.x = __builtin_amdgcn_perm(V1[0][pp], V0[0][pp], 0x05040100u); lo.y = __builtin_amdgcn_perm(V1[1][pp], V0[1][pp], 0x05040100u);
      hi.x = __builtin_amdgcn_perm(V1[0][pp], V0[0][pp], 0x07060302u); hi.y = __builtin_amdgcn_perm(V1[1][pp], V0[1][pp], 0x07060302u);
      *(u32x2*)(fp + pp * 32) = lo; *(u32x2*)(fp + pp * 32 + 16) = hi;
      const unsigned by = NI[0][pp] | (NI[1][pp] << 4);
      char* q = stg + 2048 + (fl0 + 2 * pp) * 8 + nbp * 2 + jp;
      *(unsigned char*)q = (unsigned char)by; *(unsigned char*)(q + 8) = (unsigned char)(by >> 16);
    }
    int tot = 0;
    for (unsigned rem = emask;;) { const u64 bl = __ballot(rem != 0); if (!bl) break; tot += __popcll(bl); rem &= rem - 1; }
    if (lane == 0) { *(int*)(img + PL_WT + wv * 4) = tot; asm volatile("s_waitcnt lgkmcnt(0)" ::: "memory"); LDS_ADD(PL_CT); }
    asm volatile("" ::: "memory");
    const unsigned voff = (unsigned)((wv >> 1) * 4096 + (wv & 1) * 512 + (lane >> 5) * 1024 + (lane & 31) * 16);
    __builtin_amdgcn_raw_buffer_store_b128(*(const u32x4*)(stg + l16), rs, PC_VAL + voff, pdst, SC1);
    __builtin_amdgcn_raw_buffer_store_b128(*(const u32x4*)(stg + 1024 + l16), rs, PC_VAL + 2048 + voff, pdst, SC1);
    __builtin_amdgcn_raw_buffer_store_b32(*(const unsigned*)(stg + 2048 + lane * 4), rs, PC_IDX + wv * 256 + lane * 4, pdst, SC1);
    while (LDS_LOAD(PL_CT) < 4u) {}
    asm volatile("" ::: "memory");
    const u32x4 wt = *(const u32x4*)(img + PL_WT);
    const int total = (int)(wt.x + wt.y + wt.z + wt.w);
    unsigned epp;
    asm volatile("s_waitcnt vmcnt(%2)\n\tv_mov_b32 %0, %1" : "=v"(epp) : "v"(ep), "n"(XP + 3) : "memory");
    const unsigned rtag = (epp + 1u) * 0x9E3779B1u ^ 0x7F4A7C15u;
    if (wv == 0 && lane >= total && lane < 24) __builtin_amdgcn_raw_buffer_store_b64((u32x2){0u, rtag}, rs, PC_TR + lane * 8, pdst, SC1);
    unsigned s2mask = 0;
    int base = (wv > 0 ? (int)wt.x : 0) + (wv > 1 ? (int)wt.y : 0) + (wv > 2 ? (int)wt.z : 0);
    for (unsigned rem = emask;;) {
      const u64 bl = __ballot(rem != 0); if (!bl) break;
      if (rem != 0) {
        const int q = base + __builtin_amdgcn_mbcnt_hi((unsigned)(bl >> 32), __builtin_amdgcn_mbcnt_lo((unsigned)bl, 0));
        const int bit = __builtin_ctz(rem);
        if (q < 24) {
          const int g = bit >> 3, pp = (bit >> 2) & 1, e = (bit >> 1) & 1, hf = bit & 1;
          unsigned dv = 0;
#pragma unroll
          for (int gg = 0; gg < 2; ++gg)
#pragma unroll
            for (int ee = 0; ee < 2; ++ee)
#pragma unroll
              for (int p2 = 0; p2 < 2; ++p2) dv = (g == gg && e == ee && pp == p2) ? r[gg * 4 + 1 + ee][p2] : dv;
          const unsigned val = hf ? (dv >> 16) : (dv & 0xFFFFu);
          const unsigned gi = (unsigned)(kb * 32 + gp * 2 + g);
          const unsigned rowid = (unsigned)(nbp * 16 + 4 * (o >> 2) + 2 * pp + hf);
          __builtin_amdgcn_raw_buffer_store_b64((u32x2){(val << 16) | ((unsigned)e << 15) | (rowid << 8) | gi, rtag}, rs, PC_TR + q * 8, pdst, SC1);
        } else s2mask |= 1u << bit;
      }
      base += __popcll(bl); rem &= rem - 1;
    }
    unsigned nbm = 0;
    if (total > 24) {
      const u64 bl = __ballot(s2mask != 0);
#pragma unroll
      for (int i = 0; i < 4; ++i) nbm |= (bl & (0x1111111111111111ull << i)) ? (1u << i) : 0u;
#pragma unroll
      for (int pp = 0; pp < 2; ++pp) {
        unsigned wv2[2][2];
#pragma unroll
        for (int g = 0; g < 2; ++g) {
          const unsigned bb = r[g * 4 + 1][pp], cc = r[g * 4 + 2][pp];
          const unsigned bits = (s2mask >> (g * 8 + pp * 4)) & 15u;
          wv2[0][g] = ((bits & 1u) ? (bb & 0xFFFFu) : 0u) | ((bits & 4u) ? (cc << 16) : 0u);
          wv2[1][g] = ((bits & 2u) ? (bb >> 16) : 0u) | ((bits & 8u) ? (cc & 0xFFFF0000u) : 0u);
        }
        *(u32x2*)(fp + pp * 32) = (u32x2){wv2[0][0], wv2[0][1]}; *(u32x2*)(fp + pp * 32 + 16) = (u32x2){wv2[1][0], wv2[1][1]};
      }
      asm volatile("" ::: "memory");
      __builtin_amdgcn_raw_buffer_store_b128(*(const u32x4*)(stg + l16), rs, PC_S2V + voff, pdst, SC1);
      __builtin_amdgcn_raw_buffer_store_b128(*(const u32x4*)(stg + 1024 + l16), rs, PC_S2V + 2048 + voff, pdst, SC1);
    }
    if (lane == 0) __builtin_amdgcn_raw_buffer_store_b8((unsigned char)nbm, rs, PC_SM + wv, pdst, SC1);
    asm volatile("s_waitcnt vmcnt(0)" ::: "memory");
    if (lane == 0) {
      LDS_ADD(PL_XR);
      const unsigned t0 = epp + 1u;
      if (LDS_ADD(PL_DN) == 3u) __hip_atomic_store(flags + rank * FSTR, ((u64)(t0 * 0x9E3779B1u ^ 0x7F4A7C15u) << 32) | t0, __ATOMIC_RELAXED, __HIP_MEMORY_SCOPE_AGENT);
    }
  } else {
    asm volatile("s_waitcnt vmcnt(0)" ::: "memory");
    if (lane == 0) LDS_ADD(PL_XR);
  }
  asm volatile("" : "+v"(ep), "+v"(bq[0]), "+v"(bq[1]) :: "memory");
  const int pb0 = wv * 8 * PC_SIZE;
  f32x4 acc[4][4];
  auto init_acc = [&]() __attribute__((always_inline)) {
    const unsigned bw[8] = {bq[0].x, bq[0].y, bq[0].z, bq[0].w, bq[1].x, bq[1].y, bq[1].z, bq[1].w};
#pragma unroll
    for (int nb = 0; nb < 4; ++nb) {
      const f32x4 bv = {__uint_as_float(bw[2 * nb] << 16), __uint_as_float(bw[2 * nb] & 0xFFFF0000u), __uint_as_float(bw[2 * nb + 1] << 16), __uint_as_float(bw[2 * nb + 1] & 0xFFFF0000u)};
#pragma unroll
      for (int mb = 0; mb < 4; ++mb) acc[nb][mb] = bv;
    }
  };
  init_acc();
  const unsigned rowb = ml * 1024;
  const unsigned u = (unsigned)(lq ^ ml);
#define WAIT_X() do { while (LDS_LOAD(PL_XR) < 8u) __builtin_amdgcn_s_sleep(1); asm volatile("" ::: "memory"); } while (0)

  const unsigned tg0 = ep + 1u, tg1 = tg0 * 0x9E3779B1u ^ 0x7F4A7C15u;
  unsigned rec[3];
  const unsigned tb = rowb + (unsigned)(ml << 4);
#define GATH(G_, AD_, OFF_) asm volatile("ds_read_b64 %0, %8 offset:%16\n\tds_read_b64 %1, %9 offset:%16\n\tds_read_b64 %2, %10 offset:%16\n\tds_read_b64 %3, %11 offset:%16\n\t" \
        "ds_read_b64 %4, %12 offset:%16\n\tds_read_b64 %5, %13 offset:%16\n\tds_read_b64 %6, %14 offset:%16\n\tds_read_b64 %7, %15 offset:%16" \
        : "=&v"(G_[0]), "=&v"(G_[1]), "=&v"(G_[2]), "=&v"(G_[3]), "=&v"(G_[4]), "=&v"(G_[5]), "=&v"(G_[6]), "=&v"(G_[7]) \
        : "v"(AD_[0]), "v"(AD_[1]), "v"(AD_[2]), "v"(AD_[3]), "v"(AD_[4]), "v"(AD_[5]), "v"(AD_[6]), "v"(AD_[7]), "n"(OFF_) : "memory")
#define GWAIT(G_, N_) asm volatile("s_waitcnt lgkmcnt(%8)" : "+v"(G_[0]), "+v"(G_[1]), "+v"(G_[2]), "+v"(G_[3]), "+v"(G_[4]), "+v"(G_[5]), "+v"(G_[6]), "+v"(G_[7]) : "n"(N_) : "memory")
#define BATCH_SETUP(BT, AD_, AT_, TI_) do { \
      const int ri_ = (BT) >> 1; \
      const bool mine_ = (lq >> 1) == ((BT) & 1); \
      const u32x4 k0_ = *(const u32x4*)(stg + 2048 + (BT) * 128 + lq * 32), k1_ = *(const u32x4*)(stg + 2048 + (BT) * 128 + lq * 32 + 16); \
      _Pragma("unroll") for (int hf = 0; hf < 2; ++hf) { \
          \
        const unsigned aw = (mine_ && ((rec[ri_] >> 13) & 1u) == (unsigned)hf) ? ap[ri_] : 2816u + (unsigned)lane * 2u; \
        *(u16*)(stg + aw) = (u16)(rec[ri_] >> 16); \
        asm volatile("" ::: "memory"); \
        AT_[2 * hf] = *(const u32x4*)(stg + l16); AT_[2 * hf + 1] = *(const u32x4*)(stg + 1024 + l16); \
        asm volatile("" ::: "memory"); \
        *(u16*)(stg + aw) = (u16)0; \
      } \
      const unsigned c32 = (unsigned)(cls[ri_] >> (32 * ((BT) & 1))) >> (16 * (lq >> 1) + 2 * (lq & 1)); \
      const unsigned lo4 = c32 & 0x1111u, hi4 = (c32 >> 1) & 0x1111u;            \
      const unsigned l2 = (lo4 | (lo4 >> 2)) & 0x0505u, h2 = (hi4 | (hi4 >> 2)) & 0x0505u; \
      TI_ = 0x8888u | ((l2 | (l2 >> 4)) & 0x55u) | (((h2 | (h2 >> 4)) & 0x55u) << 8); \
      AD_[0] = tb ^ k0_.x; AD_[1] = tb ^ k0_.y; AD_[2] = tb ^ k0_.z; AD_[3] = tb ^ k0_.w; AD_[4] = tb ^ k1_.x; AD_[5] = tb ^ k1_.y; AD_[6] = tb ^ k1_.z; AD_[7] = tb ^ k1_.w; \
    } while (0)
#define APPLY(G_, AT_, TI_, MB) do { u32x8 b8; \
      _Pragma("unroll") for (int j = 0; j < 8; ++j) b8[j] = __builtin_amdgcn_alignbit(G_[j].y, G_[j].x, 16); \
      _Pragma("unroll") for (int nb = 0; nb < 4; ++nb) acc[nb][MB] = SMF(AT_[nb], b8, acc[nb][MB], TI_, 0); } while (0)
#define TIE_APPLY(B0, B1) do { \
      const unsigned jr = (unsigned)((lq & 1) * 4 + (ml >> 2)), sq = (unsigned)(ml & 3); \
      const unsigned qa = (jr >> 2) * 2u + (sq >> 1), el = 4u * (sq & 1u) + (jr & 3u);        \
      unsigned ap[3]; u64 cls[3]; \
      if ((B0) == 0) { *(u32x4*)(stg + l16) = (u32x4){0, 0, 0, 0}; *(u32x4*)(stg + 1024 + l16) = (u32x4){0, 0, 0, 0}; } \
      _Pragma("unroll") for (int i = 0; i < 3; ++i) { \
        const unsigned gi = rec[i] & 255u; \
        if ((B0) == 0) *(unsigned*)(stg + 2048 + (((i * 2 + (lq >> 1)) * 4 + sq) * 8 + jr) * 4) = ((gi & 128u) << 9) | ((gi & 127u) << 3); \
        ap[i] = ((rec[i] >> 12) & 1u) * 1024u + (((rec[i] >> 8) & 15u) + 16u * qa) * 16u + el * 2u; \
        cls[i] = __ballot((rec[i] >> 15) & 1u); \
      } \
      asm volatile("" ::: "memory"); \
      unsigned ad[2][8], ti[2]; u32x4 at[2][4]; u32x2 gA[8], gB[8]; \
      BATCH_SETUP(B0, ad[0], at[0], ti[0]); \
      GATH(gA, ad[0], 0); \
      _Pragma("unroll") for (int bt = (B0); bt < (B1); ++bt) { \
        const int p = (bt - (B0)) & 1; \
        GATH(gB, ad[p], 16384); GWAIT(gA, 8); APPLY(gA, at[p], ti[p], 0); \
        SB; \
        if (bt + 1 < (B1)) BATCH_SETUP(bt + 1, ad[1 - p], at[1 - p], ti[1 - p]); \
        SB; \
        GATH(gA, ad[p], 32768); GWAIT(gB, 8); APPLY(gB, at[p], ti[p], 1); \
        GATH(gB, ad[p], 49152); GWAIT(gA, 8); APPLY(gA, at[p], ti[p], 2); \
        if (bt + 1 < (B1)) { GATH(gA, ad[1 - p], 0); GWAIT(gB, 8); } else GWAIT(gB, 0); \
        APPLY(gB, at[p], ti[p], 3); \
      } \
      if ((B1) == 4) {        \
        const u64 xtra = __ballot(rec[2] != 0u); \
        _Pragma("unroll") for (int bt = 4; bt < 6; ++bt) { \
          if ((unsigned)(xtra >> (32 * (bt & 1))) == 0u) continue; \
          BATCH_SETUP(bt, ad[0], at[0], ti[0]); \
          GATH(gA, ad[0], 0); GATH(gB, ad[0], 16384); GWAIT(gA, 0); GWAIT(gB, 0); APPLY(gA, at[0], ti[0], 0); APPLY(gB, at[0], ti[0], 1); \
          GATH(gA, ad[0], 32768); GATH(gB, ad[0], 49152); GWAIT(gA, 0); GWAIT(gB, 0); APPLY(gA, at[0], ti[0], 2); APPLY(gB, at[0], ti[0], 3); \
        } \
      } \
    } while (0)
  const bool late = wv >= 4;
  bool ok = true;
  if (late) {
    WAIT_X();
    ok = false;
#pragma unroll 1
    for (int spin = 0; spin < SPIN_MAX; ++spin) {
      const u32x2 g0 = LDI2(lq * PC_SIZE + PC_TR + ml * 8, pb0), g1 = LDI2(lq * PC_SIZE + PC_TR + ml * 8, pb0 + 4 * PC_SIZE);
      const u32x2 g2 = LDI2((lane >> 3) * PC_SIZE + PC_TR + 128 + (lane & 7) * 8, pb0);
      rec[0] = g0.x; rec[1] = g1.x; rec[2] = g2.x;
      if (__ballot(g0.y == tg1 && g1.y == tg1 && g2.y == tg1) == ~0ull) { ok = true; break; }
      asm volatile("" ::: "memory");
      __builtin_amdgcn_s_sleep(1);
    }
    if (ok) TIE_APPLY(0, 4);
  }
  if (ok) {
    ok = false;
    const u64 tag = ((u64)tg1 << 32) | tg0;
#pragma unroll 1
    for (int spin = 0; spin < SPIN_MAX; ++spin) {
      const u64 f = __hip_atomic_load(flags + (wv * 8 + (lane & 7)) * FSTR, __ATOMIC_RELAXED, __HIP_MEMORY_SCOPE_AGENT);
      if (__ballot(f == tag) == ~0ull) { ok = true; break; }
      __builtin_amdgcn_s_sleep(1);
    }
    __builtin_amdgcn_fence(__ATOMIC_ACQUIRE, "wavefront");
  }
  if (ok) {
    const unsigned smk = LDI((lane & 7) * PC_SIZE + PC_SM, pb0);
    {
      const int q0 = (mt >> 1) & 3;
      const unsigned l8 = lane * 8 + PC_IDX;
      u32x4 a[4][4]; u32x2 ix[4];
      {
        const int pb = pb0 + q0 * 2 * PC_SIZE;
#pragma unroll
        for (int s = 0; s < DEPTH; ++s) {
          ix[s] = LDI2(l8 + (s & 1) * 512, pb + (s >> 1) * PC_SIZE);
#pragma unroll
          for (int nb = 0; nb < 4; ++nb) a[s][nb] = LDW(l16 + (s & 1) * 4096 + nb * 1024, pb + (s >> 1) * PC_SIZE);
        }
      }
      if (!late) {
        rec[0] = LDI(lq * PC_SIZE + PC_TR + ml * 8, pb0); rec[1] = LDI(lq * PC_SIZE + PC_TR + ml * 8, pb0 + 4 * PC_SIZE);
        rec[2] = LDI((lane >> 3) * PC_SIZE + PC_TR + 128 + (lane & 7) * 8, pb0);
        WAIT_X();
      }
      u32x8 bc[2];
      {
        const unsigned xb = rowb + (unsigned)((q0 >> 1) * 65536 + (q0 & 1) * 512);
        const unsigned a0 = xb + (u << 4), a1 = xb + ((4u ^ u) << 4);
        bc[0] = ldB(smem, a0, a1, 0); bc[1] = ldB(smem, a0, a1, 16384);
      }
#define MM(nb, mi) do { if ((nb) & 1) acc[nb][2 * hs + mi] = SMF(a[s][nb], bc[mi], acc[nb][2 * hs + mi], ix[s][(nb) >> 1], 1); \
                        else acc[nb][2 * hs + mi] = SMF(a[s][nb], bc[mi], acc[nb][2 * hs + mi], ix[s][(nb) >> 1], 0); } while (0)
#define STEP(LAST) \
      _Pragma("unroll") for (int s = 0; s < 4; ++s) { \
        const int sp = s + DEPTH, pq = (sp < 4 ? pb : pbn) + ((sp & 3) >> 1) * PC_SIZE; \
        const bool pf = sp < 4 || !(LAST); \
        u32x4 an[4]; u32x2 in; \
        _Pragma("unroll") for (int hs = 0; hs < 2; ++hs) { \
            \
          const int sn = hs ? (s + 1) & 3 : s; \
          const bool nx = hs == 0 || s < 3 || !(LAST); \
          const unsigned a0 = (hs && s == 3) ? xn0 : xa[2 * (sn & 1)], a1 = (hs && s == 3) ? xn1 : xa[2 * (sn & 1) + 1]; \
          const int xo = (sn >> 1) * 256 + (hs ? 0 : 32768); \
          u32x8 bn[2]; \
          SB; \
          MM(0, 0); MM(1, 0); \
          if (nx) bn[0] = ldB(smem, a0, a1, xo); \
          SB; \
          MM(2, 0); MM(3, 0); \
          if (nx) bn[1] = ldB(smem, a0, a1, xo + 16384); \
          SB; \
          MM(0, 1); MM(1, 1); \
          if (pf && hs == 0) in = LDI2(l8 + (sp & 1) * 512, pq); \
          if (pf) an[2 * hs] = LDW(l16 + (sp & 1) * 4096 + (2 * hs) * 1024, pq); \
          SB; \
          MM(2, 1); MM(3, 1); \
          if (pf) an[2 * hs + 1] = LDW(l16 + (sp & 1) * 4096 + (2 * hs + 1) * 1024, pq); \
          if (nx) { bc[0] = bn[0]; bc[1] = bn[1]; } \
        } \
        if (pf) { _Pragma("unroll") for (int nb = 0; nb < 4; ++nb) a[sp & 3][nb] = an[nb]; ix[sp & 3] = in; } \
      }
#define ITER_SETUP \
        const unsigned xb = rowb + (unsigned)((kq >> 1) * 65536 + (kq & 1) * 512); \
        const unsigned xbn = rowb + (unsigned)((kqn >> 1) * 65536 + (kqn & 1) * 512); \
        unsigned xa[4]; _Pragma("unroll") for (int c = 0; c < 4; ++c) xa[c] = xb + (((unsigned)(4 * c) ^ u) << 4); \
        const unsigned xn0 = xbn + (u << 4), xn1 = xbn + ((4u ^ u) << 4);
#pragma unroll 1
      for (int it = 0; it < 3; ++it) {
        const int kq = (q0 + it) & 3, kqn = (kq + 1) & 3;
        const int pb = pb0 + kq * 2 * PC_SIZE, pbn = pb0 + kqn * 2 * PC_SIZE;
        ITER_SETUP
        STEP(false)
      }
      {
        const int kq = (q0 + 3) & 3, kqn = kq;
        const int pb = pb0 + kq * 2 * PC_SIZE, pbn = pb;
        ITER_SETUP
        STEP(true)
      }
      SB;
    }
    if (__ballot(smk != 0u)) {
#pragma unroll 1
      for (int S = 0; S < 16; ++S) {
        const unsigned mw = (unsigned)__builtin_amdgcn_readlane((int)smk, S >> 1) >> (16 * (S & 1));
        const unsigned m4 = (mw | (mw >> 8)) & 15u;
        if (m4) {
          const unsigned xb = rowb + (unsigned)((S >> 3) * 65536 + ((S & 7) >> 1) * 256);
          const unsigned c0 = (unsigned)(8 * (S & 1));
          const unsigned a0 = xb + ((c0 ^ u) << 4), a1 = xb + (((c0 + 4) ^ u) << 4);
#pragma unroll
          for (int nb = 0; nb < 4; ++nb) {
            if ((m4 >> nb) & 1u) {
              const u32x4 a2 = LDW(l16 + PC_S2V + nb * 1024, pb0 + (S >> 1) * PC_SIZE + (S & 1) * 4096);
#pragma unroll
              for (int mb = 0; mb < 4; ++mb) acc[nb][mb] = SMF(a2, ldB(smem, a0, a1, mb * 16384), acc[nb][mb], 0x99999999u, 0);
            }
          }
        }
      }
    }
    if (!late) TIE_APPLY(0, 4);
  }
  if (!ok) {
    WAIT_X();
    init_acc();
#pragma unroll 1
    for (int S = 0; S < 16; ++S) {
      const unsigned xb = rowb + (unsigned)((S >> 3) * 65536 + ((S & 7) >> 1) * 256);
      const unsigned c0 = (unsigned)(8 * (S & 1));
      const unsigned a0 = xb + ((c0 ^ u) << 4), a1 = xb + (((c0 + 4) ^ u) << 4);
#pragma unroll
      for (int nb = 0; nb < 4; ++nb) {
        const u16* src = w + (size_t)(S * 64 + lq * 16) * Ntot + n0 + 16 * (ml >> 2) + 4 * nb + (ml & 3);
        u32x4 am, a2; unsigned iw = 0;
#pragma unroll
        for (int gg = 0; gg < 2; ++gg) {
          unsigned e[4];
#pragma unroll
          for (int i = 0; i < 4; ++i) e[i] = (unsigned)src[(size_t)(gg * 8 + i) * Ntot] | ((unsigned)src[(size_t)(gg * 8 + 4 + i) * Ntot] << 16);
          unsigned v0, v1, ni, eb, ec;
          prune2(e[0], e[1], e[2], e[3], v0, v1, ni, eb, ec);
          am[2 * gg] = __builtin_amdgcn_perm(v1, v0, 0x05040100u); am[2 * gg + 1] = __builtin_amdgcn_perm(v1, v0, 0x07060302u);
          const unsigned xb2 = e[1] & eb, xc2 = e[2] & ec;
          a2[2 * gg] = __builtin_amdgcn_perm(xc2, xb2, 0x05040100u); a2[2 * gg + 1] = __builtin_amdgcn_perm(xc2, xb2, 0x07060302u);
          iw |= ((ni & 15u) | ((ni >> 12) & 0xF0u)) << (8 * gg);
        }
#pragma unroll
        for (int mb = 0; mb < 4; ++mb) {
          const u32x8 b = ldB(smem, a0, a1, mb * 16384);
          acc[nb][mb] = SMF(am, b, acc[nb][mb], iw, 0);
          acc[nb][mb] = SMF(a2, b, acc[nb][mb], 0x99999999u, 0);
        }
      }
    }
  }
  {
    u16* ob = out + (size_t)(mt * 64) * Ntot + n0;
#pragma unroll
    for (int mb = 0; mb < 4; ++mb) {
#pragma unroll
      for (int hf = 0; hf < 2; ++hf) {
        u32x4 v;
#pragma unroll
        for (int i = 0; i < 4; ++i) {
          const f32x4 c = acc[2 * hf + (i >> 1)][mb];
          const f32x2 f = {c[2 * (i & 1)], c[2 * (i & 1) + 1]};
          v[i] = __builtin_bit_cast(unsigned, __builtin_convertvector(f, bf16x2));
        }
        const int ch = lq * 2 + hf;
        *(u32x4*)(stg + ml * 128 + ((ch ^ (ml & 7)) << 4)) = v;
      }
      asm volatile("" ::: "memory");
#pragma unroll
      for (int i = 0; i < 2; ++i) {
        const int row = (lane >> 3) + 8 * i;
        const u32x4 v = *(const u32x4*)(stg + row * 128 + (((lane & 7) ^ (row & 7)) << 4));
        __builtin_nontemporal_store(v, (u32x4*)(ob + (size_t)(mb * 16 + row) * Ntot + (lane & 7) * 8));
      }
      asm volatile("" ::: "memory");
    }
  }
  if (tid == 0) __hip_atomic_fetch_add(epoch, 1u, __ATOMIC_RELAXED, __HIP_MEMORY_SCOPE_AGENT);
}

extern "C" void kernel_launch(void* const* d_in, const int* in_sizes, int n_in, void* d_out, int out_size, void* d_ws, size_t ws_size, hipStream_t stream) {
  (void)hipFuncSetAttribute((const void*)sp24_gemm, hipFuncAttributeMaxDynamicSharedMemorySize, 163840);
  sp24_gemm<<<256, 512, 163840, stream>>>((const u16*)d_in[0], (const u16*)d_in[1], (const u16*)d_in[2], (u16*)d_out, (char*)d_ws);
}
```
